```python
import torch

def _softmax(x, axis=-1):
    return torch.softmax(x, dim=axis)
def _gelu(x):
    return torch.nn.functional.gelu(x, approximate="tanh")
def _silu(x):
    return torch.nn.functional.silu(x)
def _sigmoid(x):
    return torch.sigmoid(x)
def _layernorm(x, axis=-1, eps=1e-5):
    m = x.mean(dim=axis, keepdim=True)
    v = x.var(dim=axis, keepdim=True, unbiased=False)
    return (x - m) / torch.sqrt(v + eps)
def _scatter_add(tgt, idx, src, axis=0):
    return tgt.clone().index_add_(axis, idx, src)
def _segment_sum(src, seg, n):
    out = torch.zeros((n,) + src.shape[1:], dtype=src.dtype)
    return out.index_add_(0, seg, src)
def _pad(x, pw):
    flat = tuple(v for pair in reversed(pw) for v in pair)
    return torch.nn.functional.pad(x, flat)

kernel_name = 'interleave_layers__spectral_gfnet__dense_activation_gelu__prompt'

def setup_inputs():

    _w = (lambda a: (lambda t: t.to(torch.bfloat16) if t.is_floating_point() else t)(torch.as_tensor(a)))
    def _A_setup():
        B, S, D = 1, 2048, 1024
        Df = D // 2 + 1
        return {
            "x": _w((lambda *s: torch.randn(*s, dtype=torch.bfloat16) * 0.05)(B, S, D)),
            "w_r": _w((lambda *s: torch.randn(*s, dtype=torch.bfloat16) * 0.4)(S, Df) * 0.02),
            "w_i": _w((lambda *s: torch.randn(*s, dtype=torch.bfloat16) * 0.4)(S, Df) * 0.02),
        }
    _A_in = _A_setup()

    def _B_setup():
        B, S, D, F = 1, 2048, 1024, 3584
        return {
            "x": _w((lambda *s: torch.randn(*s, dtype=torch.bfloat16) * 0.05)(B, S, D)),
            "w1": _w((lambda *s: torch.randn(*s, dtype=torch.bfloat16) * 0.4)(D, F) * (D ** -0.5)),
            "b1": _w((lambda *s: torch.randn(*s, dtype=torch.bfloat16) * 12.8)(F) * 0.01),
            "w2": _w((lambda *s: torch.randn(*s, dtype=torch.bfloat16) * 0.1)(F, D) * (F ** -0.5)),
            "b2": _w((lambda *s: torch.randn(*s, dtype=torch.bfloat16) * 0.8)(D) * 0.01),
        }
    _B_in = _B_setup()

    return {
        "A_x": _A_in["x"],
        "A_w_r": _A_in["w_r"],
        "A_w_i": _A_in["w_i"],
        "B_x": _B_in["x"],
        "B_w1": _B_in["w1"],
        "B_b1": _B_in["b1"],
        "B_w2": _B_in["w2"],
        "B_b2": _B_in["b2"],
    }

def reference(A_x, A_w_r, A_w_i, B_x, B_w1, B_b1, B_w2, B_b2):
    def _A_ref(x, w_r, w_i):
        B, S, D = x.shape
        X = (lambda x, axes=(-2,-1): torch.fft.rfft2(x.float(), dim=axes))(x, axes=(-2, -1))
        W = w_r + 1j * w_i
        X = X * W
        y = (lambda x, s, axes=(-2,-1): torch.fft.irfft2(x, s=s, dim=axes).to(torch.bfloat16))(X, (S, D), axes=(-2, -1))
        return y

    def _B_ref(x, w1, b1, w2, b2):
        h = torch.einsum("bsd,df->bsf", x, w1) + b1
        h = _gelu(h)
        return torch.einsum("bsf,fd->bsd", h, w2) + b2

    x = A_x
    x = x + _A_ref(_layernorm(x, axis=-1), A_w_r, A_w_i)
    x = x + _B_ref(_layernorm(x, axis=-1), B_w1, B_b1, B_w2, B_b2)
    x = x + _A_ref(_layernorm(x, axis=-1), A_w_r, A_w_i)
    x = x + _B_ref(_layernorm(x, axis=-1), B_w1, B_b1, B_w2, B_b2)
    return x
```

```cpp
#include <hip/hip_runtime.h>
#include <hip/hip_bf16.h>
#include <stdint.h>

#define DI __device__ __forceinline__
#define LAS __attribute__((address_space(3)))
#define GAS __attribute__((address_space(1)))
typedef __bf16 bf16x8 __attribute__((ext_vector_type(8)));
typedef __bf16 bf16x2 __attribute__((ext_vector_type(2)));
typedef short s16x4 __attribute__((ext_vector_type(4)));
typedef float f32x4 __attribute__((ext_vector_type(4)));
typedef float f32x2 __attribute__((ext_vector_type(2)));
typedef unsigned u32x4 __attribute__((ext_vector_type(4)));
typedef unsigned u32x2 __attribute__((ext_vector_type(2)));
typedef unsigned short u16;

constexpr int S_ = 2048, D_ = 1024, F_ = 3584;

DI unsigned pk(float lo, float hi) { f32x2 v = {lo, hi}; return __builtin_bit_cast(unsigned, __builtin_convertvector(v, bf16x2)); }
DI float bflo(unsigned u) { return __uint_as_float(u << 16); }
DI float bfhi(unsigned u) { return __uint_as_float(u & 0xffff0000u); }
DI float rbf(float x) { return bflo(pk(x, x)); }
DI f32x4 mfma(u32x4 a, u32x4 b, f32x4 c) {
  return __builtin_amdgcn_mfma_f32_16x16x32_bf16(__builtin_bit_cast(bf16x8, a), __builtin_bit_cast(bf16x8, b), c, 0, 0, 0);
}
DI float cosr(float rev) { return __builtin_amdgcn_cosf(rev); }
DI float sinr(float rev) { return __builtin_amdgcn_sinf(rev); }
#define WAIT_V(n) asm volatile("s_waitcnt vmcnt(%0)" ::"n"(n) : "memory")
#define WAIT_L(n) asm volatile("s_waitcnt lgkmcnt(%0)" ::"n"(n) : "memory")
#define BAR() __builtin_amdgcn_s_barrier()
#define SCHED() __builtin_amdgcn_sched_barrier(0)

template <int CTRL> DI float dpp_add(float v) {
  return v + __uint_as_float(__builtin_amdgcn_update_dpp(0u, __float_as_uint(v), CTRL, 0xf, 0xf, true));
}
DI float wave_sum(float v) {
  v = dpp_add<0xB1>(v);
  v = dpp_add<0x4E>(v);
  v = dpp_add<0x141>(v);
  v = dpp_add<0x140>(v);
  float a = __uint_as_float(__builtin_amdgcn_readlane(__float_as_uint(v), 0));
  float b = __uint_as_float(__builtin_amdgcn_readlane(__float_as_uint(v), 16));
  float c = __uint_as_float(__builtin_amdgcn_readlane(__float_as_uint(v), 32));
  float d = __uint_as_float(__builtin_amdgcn_readlane(__float_as_uint(v), 48));
  return (a + b) + (c + d);
}
DI void ln_stats(const float (&v)[16], float& mean, float& rstd) {
  float s = 0.f;
#pragma unroll
  for (int i = 0; i < 16; ++i) s += v[i];
  const float m = wave_sum(s) * (1.0f / 1024.0f);
  float q = 0.f;
#pragma unroll
  for (int i = 0; i < 16; ++i) { const float d = v[i] - m; q += d * d; }
  const float var = wave_sum(q) * (1.0f / 1024.0f);
  mean = rbf(m);
  const float sd = rbf(__builtin_sqrtf(rbf(rbf(var) + 1e-5f)));
  rstd = 1.0f / sd;
}

struct K1Args {
  const u16* x;
  const float* part;
  const u16* b2;
  u16* xo;
  unsigned* z;
  const u16* wr; const u16* wi;
  unsigned* wt; unsigned* wq;
};

DI void wprep_tile(const K1Args& p, int blk, char* smem) {
  const int tid = threadIdx.x, wid = tid >> 6, lane = tid & 63;
  const int kb = blk & 7, fg = blk >> 3, w = fg & 3, tg = fg >> 2;
  unsigned* L = (unsigned*)smem;
  const int k = 64 * kb + lane;
  unsigned short r[8], im[8];
#pragma unroll
  for (int i = 0; i < 8; ++i) {
    const int fi = wid * 8 + i, f = 16 * w + (fi >> 2) + 64 * (4 * tg + (fi & 3));
    r[i] = p.wr[f * 513 + k]; im[i] = p.wi[f * 513 + k];
  }
#pragma unroll
  for (int i = 0; i < 8; ++i) {
    const float sc = 4.76837158203125e-07f;
    L[lane * 68 + wid * 8 + i] = pk(bflo(r[i]) * sc, bflo(im[i]) * sc);
  }
  __syncthreads();
#pragma unroll
  for (int i = 0; i < 2; ++i) {
    const int idx = tid + 512 * i, kl = idx >> 4, f1l = idx & 15, kk = 64 * kb + kl;
    const int k1 = kk & 31, k2 = kk >> 5;
    const int t = k1 >= 16, l1 = (k1 <= 16) ? (k1 & 15) : 32 - k1;
    const int sigma = t * 256 + (l1 + 16 * (k2 >> 2)) * 4 + (k2 & 3);
    const u32x4 v = *(const u32x4*)(L + kl * 68 + f1l * 4);
    if (kk != 0) *(u32x4*)(p.wt + ((sigma * 4 + w) * 2 + (tg >> 2)) * 256 + ((tg & 3) * 16 + f1l) * 4) = v;
  }
}
DI void wprep_special(const K1Args& p) {
  const int tid = threadIdx.x;
#pragma unroll
  for (int i = 0; i < 4; ++i) {
    const int idx = tid * 4 + i;
    const int w = idx >> 9, t = (idx >> 8) & 1, lane = (idx >> 2) & 63, reg = idx & 3;
    const int f = 16 * w + (lane & 15) + 64 * (16 * t + 4 * (lane >> 4) + reg), fm = (2048 - f) & 2047;
    const float h0r = bflo(p.wr[f * 513]) + bflo(p.wr[fm * 513]), h0i = bflo(p.wi[f * 513]) - bflo(p.wi[fm * 513]);
    const float h1r = bflo(p.wr[f * 513 + 512]) + bflo(p.wr[fm * 513 + 512]), h1i = bflo(p.wi[f * 513 + 512]) - bflo(p.wi[fm * 513 + 512]);
    const float sc = 4.76837158203125e-07f * 0.25f;
    p.wt[idx] = pk((h0r + h1r) * sc, (h0i + h1i) * sc);
    p.wq[idx] = pk((h0r - h1r) * sc, (h0i - h1i) * sc);
  }
}

template <bool FIRST>
__global__ __launch_bounds__(512) void k_rowfft(K1Args p) {
  __shared__ __attribute__((aligned(16))) char smem[64 * 68 * 4];
  if (FIRST && blockIdx.x >= 256) {
    if (blockIdx.x < 512) wprep_tile(p, blockIdx.x - 256, smem); else wprep_special(p);
    return;
  }
  const int tid = threadIdx.x, wid = tid >> 6, lane = tid & 63, li = lane & 15, g = lane >> 4;
  const int beta = blockIdx.x, mu = beta * 8 + wid;
  float v[16];
  if (FIRST) {
    const int s = 256 * (beta >> 5) + 32 * wid + (beta & 31);
    const unsigned* xr = (const unsigned*)(p.x + (size_t)s * D_);
    unsigned u[8];
#pragma unroll
    for (int j = 0; j < 8; ++j) u[j] = xr[64 * j + lane];
#pragma unroll
    for (int j = 0; j < 8; ++j) { v[2 * j] = bflo(u[j]); v[2 * j + 1] = bfhi(u[j]); }
  } else {
    const int n2 = 2 * li, pos = 8 * ((n2 >> 2) & 3) + 4 * (n2 >> 4) + (n2 & 3);
    const unsigned* xr = (const unsigned*)(p.x + (size_t)mu * D_ + 32 * g + pos);
    unsigned* xo = (unsigned*)(p.xo + (size_t)mu * D_ + 32 * g + pos);
    const f32x2* p0 = (const f32x2*)(p.part + (size_t)mu * D_) + lane;
    const f32x2* p1 = (const f32x2*)(p.part + (size_t)(S_ + mu) * D_) + lane;
    const unsigned* bb = (const unsigned*)p.b2 + lane;
    unsigned u[8], b[8]; f32x2 a0[8], a1[8];
#pragma unroll
    for (int j = 0; j < 8; ++j) { u[j] = xr[64 * j]; a0[j] = p0[64 * j]; a1[j] = p1[64 * j]; b[j] = bb[64 * j]; }
#pragma unroll
    for (int j = 0; j < 8; ++j) {
      const unsigned o = pk(bflo(u[j]) + (a0[j].x + a1[j].x + bflo(b[j])), bfhi(u[j]) + (a0[j].y + a1[j].y + bfhi(b[j])));
      xo[64 * j] = o; v[2 * j] = bflo(o); v[2 * j + 1] = bfhi(o);
    }
  }
  const bool sp = li == 0;
  u32x4 Fre, Fim, C0, S0, nS0, C1, nS1, nC1;
  float ta[8], tb[8], tc[8], td[8];
  {
    float c[8], s[8];
#pragma unroll
    for (int j = 0; j < 8; ++j) {
      const int n1 = 4 * j + g;
      c[j] = cosr((float)((n1 * li) & 31) * (1.0f / 32.0f));
      const float sn = -sinr((float)((n1 * li) & 31) * (1.0f / 32.0f));
      s[j] = sp ? ((n1 & 1) ? -1.0f : 1.0f) : sn;
    }
#pragma unroll
    for (int j = 0; j < 4; ++j) { Fre[j] = pk(c[2 * j], c[2 * j + 1]); Fim[j] = pk(s[2 * j], s[2 * j + 1]); }
#pragma unroll
    for (int j = 0; j < 8; ++j) {
      const int n2 = 8 * g + j;
      c[j] = cosr((float)((n2 * li) & 31) * (1.0f / 32.0f)); s[j] = sinr((float)((n2 * li) & 31) * (1.0f / 32.0f));
    }
#pragma unroll
    for (int j = 0; j < 4; ++j) { C0[j] = pk(c[2 * j], c[2 * j + 1]); S0[j] = pk(s[2 * j], s[2 * j + 1]); nS0[j] = S0[j] ^ 0x80008000u; }
#pragma unroll
    for (int j = 0; j < 8; ++j) {
      const int n2 = 8 * g + j;
      c[j] = cosr((float)((n2 * (li + 1)) & 31) * (1.0f / 32.0f)); s[j] = -sinr((float)((n2 * (li + 1)) & 31) * (1.0f / 32.0f));
    }
#pragma unroll
    for (int j = 0; j < 4; ++j) { C1[j] = pk(c[2 * j], c[2 * j + 1]); nS1[j] = pk(s[2 * j], s[2 * j + 1]); nC1[j] = C1[j] ^ 0x80008000u; }
    const int k1e = sp ? 16 : li;
#pragma unroll
    for (int j = 0; j < 8; ++j) {
      const int n2 = 8 * g + j;
      const float cc = cosr((float)(n2 * k1e) * (1.0f / 1024.0f)), ss = sinr((float)(n2 * k1e) * (1.0f / 1024.0f));
      ta[j] = sp ? 0.f : cc; tb[j] = sp ? cc : ss; tc[j] = sp ? 0.f : -ss; td[j] = sp ? -ss : cc;
    }
  }
  float mean, rstd;
  ln_stats(v, mean, rstd);
  u32x4 A0, A1;
#pragma unroll
  for (int j = 0; j < 4; ++j) {
    A0[j] = pk((v[4 * j] - mean) * rstd, (v[4 * j + 2] - mean) * rstd);
    A1[j] = pk((v[4 * j + 1] - mean) * rstd, (v[4 * j + 3] - mean) * rstd);
  }
  const f32x4 zero = {0.f, 0.f, 0.f, 0.f};
  const f32x4 r0 = mfma(A0, Fre, zero), i0 = mfma(A0, Fim, zero), r1 = mfma(A1, Fre, zero), i1 = mfma(A1, Fim, zero);
  u32x4 Bre1, Bim1, Bre0, Bim0;
#pragma unroll
  for (int r = 0; r < 4; ++r) {
    const float x0 = ta[2 * r] * r0[r] + tb[2 * r] * i0[r], y0 = tc[2 * r] * r0[r] + td[2 * r] * i0[r];
    const float x1 = ta[2 * r + 1] * r1[r] + tb[2 * r + 1] * i1[r], y1 = tc[2 * r + 1] * r1[r] + td[2 * r + 1] * i1[r];
    Bre1[r] = pk(x0, x1); Bim1[r] = pk(y0, y1);
    Bre0[r] = sp ? pk(r0[r], r1[r]) : Bre1[r]; Bim0[r] = sp ? 0u : Bim1[r];
  }
  f32x4 xr0 = mfma(C0, Bre0, zero), xi0 = mfma(nS0, Bre0, zero), xr1 = mfma(C1, Bre1, zero), xi1 = mfma(nS1, Bre1, zero);
  const f32x4 ny = mfma(C1, Bre0, zero);
  xr0 = mfma(S0, Bim0, xr0); xi0 = mfma(C0, Bim0, xi0); xr1 = mfma(nS1, Bim1, xr1); xi1 = mfma(nC1, Bim1, xi1);
  const float nyq = __uint_as_float(__builtin_amdgcn_readlane(__float_as_uint(ny[3]), 48));
  if (lane == 0) xi0[0] = nyq;
  u32x4 o0, o1;
#pragma unroll
  for (int r = 0; r < 4; ++r) { o0[r] = pk(xr0[r], xi0[r]); o1[r] = pk(xr1[r], xi1[r]); }
  unsigned* L = (unsigned*)smem;
  *(u32x4*)(L + wid * 516 + lane * 4) = o0;
  *(u32x4*)(L + wid * 516 + 256 + lane * 4) = o1;
  __syncthreads();
#pragma unroll
  for (int i = 0; i < 2; ++i) {
    const int ch = tid + 512 * i, sg = ch >> 1, jh = ch & 1;
    u32x4 o;
#pragma unroll
    for (int e = 0; e < 4; ++e) o[e] = L[(4 * jh + e) * 516 + sg];
    *(u32x4*)(p.z + (size_t)beta * 4096 + ch * 4) = o;
  }
}

__global__ __launch_bounds__(512) void k_colfft(const unsigned* __restrict__ zin, unsigned* __restrict__ zout,
                                                const unsigned* __restrict__ wt, const unsigned* __restrict__ wq) {
  __shared__ __attribute__((aligned(16))) char smem[32768];
  const int tid = threadIdx.x, wid = __builtin_amdgcn_readfirstlane(tid >> 6), lane = tid & 63, li = lane & 15, g = lane >> 4;
  const int pb = (blockIdx.x & 7) * 32 + (blockIdx.x >> 3), c = wid >> 2, w = wid & 3, sigma = 2 * pb + c;
  u32x4 Wv[2], Qv[2];
  Wv[0] = *(const u32x4*)(wt + ((sigma * 4 + w) * 2 + 0) * 256 + lane * 4);
  Wv[1] = *(const u32x4*)(wt + ((sigma * 4 + w) * 2 + 1) * 256 + lane * 4);
#pragma unroll
  for (int q = 0; q < 2; ++q) {
    const int P = (wid * 2 + q) * 64 + lane, beta = P >> 2, ch = (P & 3) ^ ((0x78 >> (2 * ((beta >> 2) & 3))) & 3);
    __builtin_amdgcn_global_load_lds((const GAS void*)(zin + (size_t)beta * 4096 + pb * 16 + ch * 4),
                                     (LAS void*)(smem + (wid * 2 + q) * 1024), 16, 0, 0);
  }
  u32x4 Ere[4], Eim[4], Gc[2], Gs[2], Gn[2];
  float t1c[8], t1s[8], t2c[8], t2s[8];
  {
    const int f1 = 16 * w + li;
#pragma unroll
    for (int k = 0; k < 4; ++k)
#pragma unroll
      for (int e = 0; e < 4; ++e) {
        const float a = (float)(((16 * k + 4 * g + e) * f1) & 63) * (1.0f / 64.0f);
        const float cc = cosr(a), ss = sinr(a);
        Ere[k][e] = pk(cc, ss); Eim[k][e] = pk(-ss, cc);
      }
#pragma unroll
    for (int t = 0; t < 2; ++t) {
      float cc[8], ss[8];
#pragma unroll
      for (int j = 0; j < 8; ++j) {
        const float a = (float)(((16 * (j >> 2) + 4 * g + (j & 3)) * (16 * t + li)) & 31) * (1.0f / 32.0f);
        cc[j] = cosr(a); ss[j] = sinr(a);
      }
#pragma unroll
      for (int j = 0; j < 4; ++j) { Gc[t][j] = pk(cc[2 * j], cc[2 * j + 1]); Gs[t][j] = pk(ss[2 * j], ss[2 * j + 1]); Gn[t][j] = Gs[t][j] ^ 0x80008000u; }
    }
#pragma unroll
    for (int j = 0; j < 8; ++j) {
      const float a = (float)((16 * (j >> 2) + 4 * g + (j & 3)) * f1) * (1.0f / 2048.0f);
      t1c[j] = cosr(a); t1s[j] = sinr(a);
      const float b = (float)((16 * (j >> 2) + li) * (16 * w + 4 * g + (j & 3))) * (1.0f / 2048.0f);
      t2c[j] = cosr(b); t2s[j] = sinr(b);
    }
  }
  WAIT_V(0);
  __syncthreads();
  const f32x4 zero = {0.f, 0.f, 0.f, 0.f};
  f32x4 dr[2] = {zero, zero}, di[2] = {zero, zero};
  {
    const char* base = smem + (32 * (g >> 1) + li) * 64 + (((2 * c + (g & 1)) ^ ((0x78 >> (2 * (li >> 2))) & 3)) << 4);
#pragma unroll
    for (int k = 0; k < 4; ++k)
#pragma unroll
      for (int h = 0; h < 2; ++h) {
        const u32x4 a = *(const u32x4*)(base + k * 4096 + h * 1024);
        dr[h] = mfma(a, Ere[k], dr[h]); di[h] = mfma(a, Eim[k], di[h]);
      }
  }
  u32x4 Bre, Bim;
#pragma unroll
  for (int h = 0; h < 2; ++h) {
    float x[4], y[4];
#pragma unroll
    for (int r = 0; r < 4; ++r) {
      x[r] = dr[h][r] * t1c[4 * h + r] + di[h][r] * t1s[4 * h + r];
      y[r] = di[h][r] * t1c[4 * h + r] - dr[h][r] * t1s[4 * h + r];
    }
    Bre[2 * h] = pk(x[0], x[1]); Bre[2 * h + 1] = pk(x[2], x[3]);
    Bim[2 * h] = pk(y[0], y[1]); Bim[2 * h + 1] = pk(y[2], y[3]);
  }
  f32x4 xr[2], xi[2];
#pragma unroll
  for (int t = 0; t < 2; ++t) {
    xr[t] = mfma(Gc[t], Bre, zero); xi[t] = mfma(Gn[t], Bre, zero);
    xr[t] = mfma(Gs[t], Bim, xr[t]); xi[t] = mfma(Gc[t], Bim, xi[t]);
  }
  float ur[8], ui[8];
#pragma unroll
  for (int t = 0; t < 2; ++t)
#pragma unroll
    for (int r = 0; r < 4; ++r) {
      const float a = bflo(Wv[t][r]), b = bfhi(Wv[t][r]);
      ur[4 * t + r] = xr[t][r] * a - xi[t][r] * b; ui[4 * t + r] = xr[t][r] * b + xi[t][r] * a;
    }
  if (pb == 0) {
    Qv[0] = *(const u32x4*)(wq + (w * 2 + 0) * 256 + lane * 4);
    Qv[1] = *(const u32x4*)(wq + (w * 2 + 1) * 256 + lane * 4);
    f32x2* X = (f32x2*)smem;
    __syncthreads();
    if (c == 0) {
#pragma unroll
      for (int t = 0; t < 2; ++t)
#pragma unroll
        for (int r = 0; r < 4; ++r) X[16 * w + li + 64 * (16 * t + 4 * g + r)] = (f32x2){xr[t][r], xi[t][r]};
    }
    __syncthreads();
    if (c == 0) {
#pragma unroll
      for (int t = 0; t < 2; ++t)
#pragma unroll
        for (int r = 0; r < 4; ++r) {
          const f32x2 m = X[(2048 - (16 * w + li + 64 * (16 * t + 4 * g + r))) & 2047];
          const float a = bflo(Qv[t][r]), b = bfhi(Qv[t][r]);
          ur[4 * t + r] += m.x * a + m.y * b; ui[4 * t + r] += m.x * b - m.y * a;
        }
    }
  }
  u32x4 Are, Aim;
#pragma unroll
  for (int j = 0; j < 4; ++j) { Are[j] = pk(ur[2 * j], ur[2 * j + 1]); Aim[j] = pk(ui[2 * j], ui[2 * j + 1]); }
  char* ex = smem + 16384 + c * 8192;
#pragma unroll
  for (int t = 0; t < 2; ++t) {
    f32x4 tr = mfma(Are, Gc[t], zero), ti = mfma(Are, Gs[t], zero);
    tr = mfma(Aim, Gn[t], tr); ti = mfma(Aim, Gc[t], ti);
    u32x4 o;
#pragma unroll
    for (int r = 0; r < 4; ++r)
      o[r] = pk(tr[r] * t2c[4 * t + r] - ti[r] * t2s[4 * t + r], -(tr[r] * t2s[4 * t + r] + ti[r] * t2c[4 * t + r]));
    *(u32x4*)(ex + (w * 2 + t) * 1024 + lane * 16) = o;
  }
  __syncthreads();
#pragma unroll
  for (int t = 0; t < 2; ++t) {
    f32x4 orr = zero, oi = zero;
#pragma unroll
    for (int k = 0; k < 4; ++k) {
      const u32x4 b = *(const u32x4*)(ex + (k * 2 + t) * 1024 + lane * 16);
      orr = mfma(Ere[k], b, orr); oi = mfma(Eim[k], b, oi);
    }
    u32x4 o;
#pragma unroll
    for (int r = 0; r < 4; ++r) o[r] = pk(orr[r], -oi[r]);
    const int beta = 32 * (2 * w + (g >> 1)) + 16 * t + li;
    *(u32x4*)(zout + (size_t)beta * 4096 + pb * 16 + (2 * c + (g & 1)) * 4) = o;
  }
}

template <bool FIRST>
__global__ __launch_bounds__(512) void k_rowifft(const unsigned* __restrict__ z, const u16* __restrict__ xin,
                                                 u16* __restrict__ x1, u16* __restrict__ xn) {
  __shared__ __attribute__((aligned(16))) char smem[8 * 516 * 4 + 16384];
  const int tid = threadIdx.x, wid = tid >> 6, lane = tid & 63, li = lane & 15, g = lane >> 4;
  const int beta = blockIdx.x, mu = beta * 8 + wid;
  u32x4 zi[2];
#pragma unroll
  for (int i = 0; i < 2; ++i) zi[i] = *(const u32x4*)(z + (size_t)beta * 4096 + (tid + 512 * i) * 4);
  u32x4 res[2];
  if (FIRST) {
    const int s = 256 * (beta >> 5) + 32 * wid + (beta & 31);
#pragma unroll
    for (int v = 0; v < 2; ++v) {
      const u32x2 a = *(const u32x2*)(xin + (size_t)s * D_ + 32 * (16 * v + li) + 4 * g);
      const u32x2 b = *(const u32x2*)(xin + (size_t)s * D_ + 32 * (16 * v + li) + 16 + 4 * g);
      res[v] = (u32x4){a.x, a.y, b.x, b.y};
    }
  } else {
#pragma unroll
    for (int v = 0; v < 2; ++v) res[v] = *(const u32x4*)(xin + (size_t)mu * D_ + 32 * (16 * v + li) + 8 * g);
  }
  u32x4 B0r[2], B0i[2], B1r[2], B1i[2], Cb[2];
  float twc[8], tws[8], hc[2], hs[2];
#pragma unroll
  for (int u = 0; u < 2; ++u) {
    const int n2 = 16 * u + li;
    float cc[5], ss[5];
#pragma unroll
    for (int e = 0; e < 5; ++e) { const float a = (float)((n2 * (4 * g + e)) & 31) * (1.0f / 32.0f); cc[e] = cosr(a); ss[e] = sinr(a); }
#pragma unroll
    for (int e = 0; e < 4; ++e) {
      B0r[u][e] = pk(cc[e], -ss[e]); B0i[u][e] = pk(ss[e], cc[e]);
      B1r[u][e] = pk(cc[e + 1], -ss[e + 1]); B1i[u][e] = pk(-ss[e + 1], -cc[e + 1]);
      const float b = (float)(n2 * (4 * g + e)) * (1.0f / 1024.0f);
      twc[4 * u + e] = cosr(b); tws[4 * u + e] = sinr(b);
      const bool z0 = (4 * g + e) == 0;
      Cb[u][e] = pk(z0 ? 1.0f : 2.0f * cc[e], z0 ? ((li & 1) ? -1.0f : 1.0f) : -2.0f * ss[e]);
    }
    hc[u] = 2.0f * cosr((float)n2 * (1.0f / 64.0f)); hs[u] = 2.0f * sinr((float)n2 * (1.0f / 64.0f));
  }
  unsigned* L = (unsigned*)smem;
#pragma unroll
  for (int i = 0; i < 2; ++i) {
    const int ch = tid + 512 * i, sg = ch >> 1, jh = ch & 1;
#pragma unroll
    for (int e = 0; e < 4; ++e) L[(4 * jh + e) * 516 + sg] = zi[i][e];
  }
  __syncthreads();
  const u32x4 X0 = *(const u32x4*)(L + wid * 516 + lane * 4), X1 = *(const u32x4*)(L + wid * 516 + 256 + lane * 4);
  const unsigned dn = __builtin_amdgcn_readfirstlane(X0[0]);
  const float dc = bflo(dn), nyq = bfhi(dn);
  const f32x4 zero = {0.f, 0.f, 0.f, 0.f};
  u32x4 A[2];
#pragma unroll
  for (int u = 0; u < 2; ++u) {
    const f32x4 p0r = mfma(X0, B0r[u], zero), p0i = mfma(X0, B0i[u], zero);
    const f32x4 vr = mfma(X1, B1r[u], p0r), vi = mfma(X1, B1i[u], p0i);
    float x[4], y[4];
#pragma unroll
    for (int r = 0; r < 4; ++r) {
      x[r] = vr[r] * twc[4 * u + r] - vi[r] * tws[4 * u + r];
      y[r] = vr[r] * tws[4 * u + r] + vi[r] * twc[4 * u + r];
    }
    const float sx = 2.0f * p0r[0] - dc + ((li & 1) ? -nyq : nyq);
    const float sy = hc[u] * (vr[0] - p0r[0]) - hs[u] * (vi[0] - p0i[0]);
    if (g == 0) { x[0] = sx; y[0] = sy; }
#pragma unroll
    for (int r = 0; r < 4; ++r) A[u][r] = pk(x[r], y[r]);
  }
  float v[16];
  u32x4 xo[2];
#pragma unroll
  for (int vv = 0; vv < 2; ++vv) {
#pragma unroll
    for (int u = 0; u < 2; ++u) {
      const f32x4 y = mfma(A[u], Cb[vv], zero);
      xo[vv][2 * u] = pk(bflo(res[vv][2 * u]) + y[0], bfhi(res[vv][2 * u]) + y[1]);
      xo[vv][2 * u + 1] = pk(bflo(res[vv][2 * u + 1]) + y[2], bfhi(res[vv][2 * u + 1]) + y[3]);
    }
    *(u32x4*)(x1 + (size_t)mu * D_ + 32 * (16 * vv + li) + 8 * g) = xo[vv];
#pragma unroll
    for (int e = 0; e < 4; ++e) { v[8 * vv + 2 * e] = bflo(xo[vv][e]); v[8 * vv + 2 * e + 1] = bfhi(xo[vv][e]); }
  }
  float mean, rstd;
  ln_stats(v, mean, rstd);
  char* T = smem + 8 * 516 * 4;
#pragma unroll
  for (int vv = 0; vv < 2; ++vv) {
    u32x4 o;
#pragma unroll
    for (int e = 0; e < 4; ++e) o[e] = pk((v[8 * vv + 2 * e] - mean) * rstd, (v[8 * vv + 2 * e + 1] - mean) * rstd);
    const int n1 = 16 * vv + li;
    *(u32x4*)(T + (((n1 * 4 + g) * 8) + (wid ^ (n1 & 7))) * 16) = o;
  }
  __syncthreads();
#pragma unroll
  for (int i = 0; i < 2; ++i) {
    const int idx = tid + 512 * i, unit = idx >> 3, n1 = unit >> 2, gg = unit & 3, j = (idx & 7) ^ (n1 & 7);
    const u32x4 o = *(const u32x4*)(T + idx * 16);
    *(u32x4*)(xn + ((size_t)((beta >> 1) * 32 + n1) * 64 + gg * 16 + (beta & 1) * 8 + j) * 8) = o;
  }
}

DI u32x2 tr_read(const char* p) {
  return __builtin_bit_cast(u32x2, __builtin_amdgcn_ds_read_tr16_b64_v4i16((LAS s16x4*)p));
}
DI float gelu_f(float x) {
  const float k1 = -2.0f * 1.4426950408889634f * 0.7978845608028654f, k2 = k1 * 0.044715f;
  const float t = x * (k1 + k2 * x * x);
  return x * __builtin_amdgcn_rcpf(1.0f + __builtin_amdgcn_exp2f(t));
}

template <int BM, int BN, int KT, int NS, int EPI>
struct Gemm {
  static constexpr int MB = BM / 32, NT = BN / 32;
  static constexpr int A_BYTES = BM * 128, PITCH = BN * 2, B_BYTES = 64 * PITCH, STAGE = A_BYTES + B_BYTES;
  static constexpr int GA = A_BYTES / 4096, GB = B_BYTES / 4096, GL = GA + GB, CPR = PITCH / 16;
  static constexpr int LDS = NS * STAGE + 1024;
  static_assert(B_BYTES % 4096 == 0 && A_BYTES % 4096 == 0, "tile");
  static DI int swz(int k) { return BN == 224 ? ((k >> 2) & 1) << 1 : (k & 7) << 1; }

  static DI void run(char* smem, const u16* A, int akb  , int rb0, int kb0, const u16* W, int ldw, int n0,
                     const u16* bias, void* out, int sk) {
    const int tid = threadIdx.x, wid = __builtin_amdgcn_readfirstlane(tid >> 6), lane = tid & 63, li = lane & 15, g = lane >> 4;
    const int wm = wid & 1, wn = wid >> 1;
    unsigned offA[GA], offB[GB];
#pragma unroll
    for (int i = 0; i < GA; ++i) { const int q = wid * GA + i; offA[i] = (unsigned)(((q >> 1) * akb + (q & 1)) * 1024 + lane * 16); }
#pragma unroll
    for (int i = 0; i < GB; ++i) { const int cidx = (wid * GB + i) * 64 + lane, r = cidx / CPR, cc = (cidx % CPR) ^ swz(r); offB[i] = (unsigned)(r * ldw * 2 + cc * 16); }
    const char* gA = (const char*)A + ((size_t)rb0 * akb + kb0) * 1024;
    const char* gB = (const char*)W + ((size_t)kb0 * 32 * ldw + n0) * 2;
    auto stage = [&](int t, int buf) {
      const char* a = gA + (size_t)t * 2048; const char* b = gB + (size_t)t * 64 * ldw * 2;
      char* l = smem + buf * STAGE;
#pragma unroll
      for (int i = 0; i < GB; ++i) __builtin_amdgcn_global_load_lds((const GAS void*)(b + offB[i]), (LAS void*)(l + A_BYTES + (wid * GB + i) * 1024), 16, 0, 0);
#pragma unroll
      for (int i = 0; i < GA; ++i) __builtin_amdgcn_global_load_lds((const GAS void*)(a + offA[i]), (LAS void*)(l + (wid * GA + i) * 1024), 16, 0, 0);
    };
    f32x4 acc[NT][MB];
    if (EPI == 0) {
      if (lane < NT * 2) __builtin_amdgcn_global_load_lds((const GAS void*)(bias + n0 + wn * (BN / 2) + lane * 8), (LAS void*)(smem + NS * STAGE + wid * 256), 16, 0, 0);
    }
#pragma unroll
    for (int s = 0; s < NS; ++s) stage(s, s);
    if (EPI == 0) {
      WAIT_V(NS * GL);
#pragma unroll
      for (int n = 0; n < NT; ++n) {
        const u32x2 b = *(const u32x2*)(smem + NS * STAGE + wid * 256 + n * 32 + g * 8);
        const f32x4 bv = {bflo(b.x), bfhi(b.x), bflo(b.y), bfhi(b.y)};
#pragma unroll
        for (int m = 0; m < MB; ++m) acc[n][m] = bv;
      }
    } else {
#pragma unroll
      for (int n = 0; n < NT; ++n)
#pragma unroll
        for (int m = 0; m < MB; ++m) acc[n][m] = (f32x4){0.f, 0.f, 0.f, 0.f};
    }
    const int aoff = wm * MB * 2048 + lane * 16;
    int boff[NT];
    {
      const int q = (lane >> 2) & 3, pp = lane & 3, r = 4 * g + q;
#pragma unroll
      for (int n = 0; n < NT; ++n) boff[n] = A_BYTES + r * PITCH + (((2 * (wn * NT + n) + (pp >> 1)) ^ swz(r)) << 4) + 8 * (pp & 1);
    }
    u32x4 Fa[2][MB], Fb[2][NT];
    auto rd = [&](int set, int buf, int ks) {
      const char* l = smem + buf * STAGE;
#pragma unroll
      for (int m = 0; m < MB; ++m) Fa[set][m] = *(const u32x4*)(l + aoff + m * 2048 + ks * 1024);
#pragma unroll
      for (int n = 0; n < NT; ++n) {
        const u32x2 lo = tr_read(l + boff[n] + ks * 32 * PITCH), hi = tr_read(l + boff[n] + ks * 32 * PITCH + 16 * PITCH);
        Fb[set][n] = (u32x4){lo.x, lo.y, hi.x, hi.y};
      }
    };
    auto mm = [&](int set) {
#pragma unroll
      for (int n = 0; n < NT; ++n)
#pragma unroll
        for (int m = 0; m < MB; ++m) acc[n][m] = mfma(Fb[set][n], Fa[set][m], acc[n][m]);
    };
    auto inter = [&]() {
#pragma unroll
      for (int i = 0; i < NT * MB; ++i) {
        __builtin_amdgcn_sched_group_barrier(0x008, 1, 0);
        __builtin_amdgcn_sched_group_barrier(0x120, 1, 0);
      }
    };
    WAIT_V((NS - 1) * GL); BAR();
    rd(0, 0, 0);
    int cur = 0;
    for (int t = 0; t < KT - NS; ++t) {
      const int nxt = cur == NS - 1 ? 0 : cur + 1;
      SCHED();
      rd(1, cur, 1); mm(0); inter();
      SCHED();
      WAIT_L(0); WAIT_V((NS - 2) * GL); BAR();
      SCHED();
      rd(0, nxt, 0); stage(t + NS, cur); mm(1); inter();
      cur = nxt;
    }
#pragma unroll
    for (int i = 0; i < NS; ++i) {
      const int nxt = cur == NS - 1 ? 0 : cur + 1;
      SCHED();
      rd(1, cur, 1); mm(0); inter();
      SCHED();
      if (i < NS - 1) {
        WAIT_L(0);
        if (i == NS - 2) WAIT_V(0); else if (i == NS - 3) WAIT_V(GL); else WAIT_V(2 * GL);
        BAR();
        SCHED();
        rd(0, nxt, 0);
      }
      mm(1); inter();
      cur = nxt;
    }
    SCHED();
    if (EPI == 0) {
      u16* H = (u16*)out;
#pragma unroll
      for (int m = 0; m < MB; ++m) {
        char* hb = (char*)H + ((size_t)(rb0 + wm * MB + m) * (F_ / 32) + n0 / 32) * 1024 + lane * 16;
        u32x2 pkd[NT];
#pragma unroll
        for (int n = 0; n < NT; ++n) pkd[n] = (u32x2){pk(gelu_f(acc[n][m][0]), gelu_f(acc[n][m][1])), pk(gelu_f(acc[n][m][2]), gelu_f(acc[n][m][3]))};
        if (wn == 0) {
#pragma unroll
          for (int n = 0; n + 1 < NT; n += 2) *(u32x4*)(hb + (n >> 1) * 1024) = (u32x4){pkd[n].x, pkd[n].y, pkd[n + 1].x, pkd[n + 1].y};
          if (NT & 1) *(u32x2*)(hb + (NT >> 1) * 1024) = pkd[NT - 1];
        } else {
          if (NT & 1) *(u32x2*)(hb + (NT >> 1) * 1024 + 8) = pkd[0];
#pragma unroll
          for (int n = (NT & 1); n + 1 < NT; n += 2) *(u32x4*)(hb + ((NT + n) >> 1) * 1024) = (u32x4){pkd[n].x, pkd[n].y, pkd[n + 1].x, pkd[n + 1].y};
        }
      }
    } else {
      float* P = (float*)out + (size_t)sk * S_ * D_;
#pragma unroll
      for (int m = 0; m < MB; ++m)
#pragma unroll
        for (int n = 0; n < NT; ++n)
          *(f32x4*)(P + (size_t)((rb0 + wm * MB + m) * 16 + li) * D_ + n0 + (wn * NT + n) * 16 + 4 * g) = acc[n][m];
    }
  }
};

typedef Gemm<128, 224, 16, 3, 0> G1;
typedef Gemm<128, 128, 28, 4, 1> G2;

__global__ __launch_bounds__(256) void k_gemm1(const u16* __restrict__ xn, const u16* __restrict__ w1, const u16* __restrict__ b1, u16* __restrict__ h) {
  extern __shared__ __attribute__((aligned(16))) char dsm[];
  const int b = blockIdx.x, x = b & 7, i = b >> 3;
  const int mt = (x >> 1) * 4 + (i & 3), nt = (x & 1) * 8 + (i >> 2);
  G1::run(dsm, xn, 32, mt * 8, 0, w1, F_, nt * 224, b1, h, 0);
}
__global__ __launch_bounds__(256) void k_gemm2(const u16* __restrict__ h, const u16* __restrict__ w2, float* __restrict__ part) {
  extern __shared__ __attribute__((aligned(16))) char dsm[];
  const int b = blockIdx.x, x = b & 7, i = b >> 3;
  const int mt = (x >> 1) * 4 + (i & 3), sk = x & 1, nt = i >> 2;
  G2::run(dsm, h, 112, mt * 8, sk * 56, w2, D_, nt * 128, nullptr, part, sk);
}

__global__ __launch_bounds__(256) void k_final(const u16* __restrict__ x3, const float* __restrict__ part, const u16* __restrict__ b2, u16* __restrict__ out) {
  const int idx = blockIdx.x * 256 + threadIdx.x, mu = idx >> 7, ch = idx & 127, n1 = ch >> 2, g = ch & 3;
  const u32x4 xv = *(const u32x4*)(x3 + (size_t)mu * D_ + ch * 8);
  const int s = (mu & ~255) + 32 * (mu & 7) + ((mu >> 3) & 31);
#pragma unroll
  for (int u = 0; u < 2; ++u) {
    const int d = 32 * n1 + 16 * u + 4 * g;
    const f32x4 a = *(const f32x4*)(part + (size_t)mu * D_ + d), b = *(const f32x4*)(part + (size_t)(S_ + mu) * D_ + d);
    const u32x2 bb = *(const u32x2*)(b2 + d);
    u32x2 o;
    o.x = pk(bflo(xv[2 * u]) + (a[0] + b[0] + bflo(bb.x)), bfhi(xv[2 * u]) + (a[1] + b[1] + bfhi(bb.x)));
    o.y = pk(bflo(xv[2 * u + 1]) + (a[2] + b[2] + bflo(bb.y)), bfhi(xv[2 * u + 1]) + (a[3] + b[3] + bfhi(bb.y)));
    *(u32x2*)(out + (size_t)s * D_ + d) = o;
  }
}

extern "C" void kernel_launch(void* const* d_in, const int* in_sizes, int n_in, void* d_out, int out_size, void* d_ws, size_t ws_size,
                              hipStream_t stream) {
  const u16* x = (const u16*)d_in[0]; const u16* wr = (const u16*)d_in[1]; const u16* wi = (const u16*)d_in[2];
  const u16* w1 = (const u16*)d_in[4]; const u16* b1 = (const u16*)d_in[5]; const u16* w2 = (const u16*)d_in[6]; const u16* b2 = (const u16*)d_in[7];
  char* ws = (char*)d_ws;
  const size_t MB = 1u << 20;
  unsigned* wt = (unsigned*)(ws); unsigned* wq = (unsigned*)(ws + 4 * MB);
  unsigned* z1 = (unsigned*)(ws + 5 * MB); unsigned* z2 = (unsigned*)(ws + 9 * MB);
  u16* x1 = (u16*)(ws + 13 * MB); u16* x2 = (u16*)(ws + 17 * MB); u16* x3 = (u16*)(ws + 21 * MB);
  u16* xn = (u16*)(ws + 25 * MB); u16* h = (u16*)(ws + 29 * MB); float* part = (float*)(ws + 44 * MB);
  hipFuncSetAttribute((const void*)k_gemm1, hipFuncAttributeMaxDynamicSharedMemorySize, G1::LDS);
  hipFuncSetAttribute((const void*)k_gemm2, hipFuncAttributeMaxDynamicSharedMemorySize, G2::LDS);
  K1Args a; a.x = x; a.part = part; a.b2 = b2; a.xo = x2; a.z = z1; a.wr = wr; a.wi = wi; a.wt = wt; a.wq = wq;
  k_rowfft<true><<<513, 512, 0, stream>>>(a);
  k_colfft<<<256, 512, 0, stream>>>(z1, z2, wt, wq);
  k_rowifft<true><<<256, 512, 0, stream>>>(z2, x, x1, xn);
  k_gemm1<<<256, 256, G1::LDS, stream>>>(xn, w1, b1, h);
  k_gemm2<<<256, 256, G2::LDS, stream>>>(h, w2, part);
  a.x = x1;
  k_rowfft<false><<<256, 512, 0, stream>>>(a);
  k_colfft<<<256, 512, 0, stream>>>(z1, z2, wt, wq);
  k_rowifft<false><<<256, 512, 0, stream>>>(z2, x2, x3, xn);
  k_gemm1<<<256, 256, G1::LDS, stream>>>(xn, w1, b1, h);
  k_gemm2<<<256, 256, G2::LDS, stream>>>(h, w2, part);
  k_final<<<1024, 256, 0, stream>>>(x3, part, b2, (u16*)d_out);
}
```

```cpp
#include <hip/hip_runtime.h>
#include <hip/hip_bf16.h>
#include <stdint.h>
#include <type_traits>

#define DI __device__ __forceinline__
#define LAS __attribute__((address_space(3)))
#define GAS __attribute__((address_space(1)))
typedef __bf16 bf16x8 __attribute__((ext_vector_type(8)));
typedef __bf16 bf16x2 __attribute__((ext_vector_type(2)));
typedef short s16x4 __attribute__((ext_vector_type(4)));
typedef float f32x4 __attribute__((ext_vector_type(4)));
typedef float f32x2 __attribute__((ext_vector_type(2)));
typedef unsigned u32x4 __attribute__((ext_vector_type(4)));
typedef unsigned u32x2 __attribute__((ext_vector_type(2)));
typedef unsigned short u16;
typedef __amdgpu_buffer_rsrc_t rsrc_t;
template <int V> using IC = std::integral_constant<int, V>;

constexpr int S_ = 2048, D_ = 1024, F_ = 3584;

DI unsigned pk2(f32x2 v) { return __builtin_bit_cast(unsigned, __builtin_convertvector(v, bf16x2)); }
DI unsigned pk(float lo, float hi) { return pk2((f32x2){lo, hi}); }
DI float bflo(unsigned u) { return __uint_as_float(u << 16); }
DI float bfhi(unsigned u) { return __uint_as_float(u & 0xffff0000u); }
DI float rbf(float x) { return bflo(pk(x, x)); }
DI f32x4 mfma(u32x4 a, u32x4 b, f32x4 c) {
  return __builtin_amdgcn_mfma_f32_16x16x32_bf16(__builtin_bit_cast(bf16x8, a), __builtin_bit_cast(bf16x8, b), c, 0, 0, 0);
}
DI f32x2 lo2(f32x4 a) { return __builtin_shufflevector(a, a, 0, 1); }
DI f32x2 hi2(f32x4 a) { return __builtin_shufflevector(a, a, 2, 3); }
DI f32x4 mad2(f32x4 a, f32x4 b, f32x4 c, f32x4 d) {
  const f32x2 l = __builtin_elementwise_fma(lo2(a), lo2(b), lo2(c) * lo2(d)), h = __builtin_elementwise_fma(hi2(a), hi2(b), hi2(c) * hi2(d));
  return __builtin_shufflevector(l, h, 0, 1, 2, 3);
}
DI u32x4 zip4(f32x4 x, f32x4 y) { return (u32x4){pk(x[0], y[0]), pk(x[1], y[1]), pk(x[2], y[2]), pk(x[3], y[3])}; }
DI u32x4 cat4(f32x4 x, f32x4 y) { return (u32x4){pk2(lo2(x)), pk2(hi2(x)), pk2(lo2(y)), pk2(hi2(y))}; }
DI float cosr(float rev) { return __builtin_amdgcn_cosf(rev); }
DI float sinr(float rev) { return __builtin_amdgcn_sinf(rev); }
#define WAIT_V(n) asm volatile("s_waitcnt vmcnt(%0)" ::"n"(n) : "memory")
#define WAIT_L0() __builtin_amdgcn_s_waitcnt(0xC07F)
#define FENCE() asm volatile("" ::: "memory")
#define BAR() do { FENCE(); __builtin_amdgcn_s_barrier(); FENCE(); } while (0)
#define LBAR() do { WAIT_L0(); BAR(); } while (0)
#define SCHED() __builtin_amdgcn_sched_barrier(0)
template <int I, int N, class F> DI void sfor(F&& f) { if constexpr (I < N) { f(IC<I>{}); sfor<I + 1, N>(f); } }
template <class T> DI void pin(T& x) { asm volatile("" : "+v"(x)); }
DI u32x2 tr_read(const LAS char* p) { return __builtin_bit_cast(u32x2, __builtin_amdgcn_ds_read_tr16_b64_v4i16((LAS s16x4*)p)); }
DI void set_m0(unsigned base, int imm) { asm volatile("s_add_u32 m0, %0, %1\n\ts_nop 0" ::"s"(base), "i"(imm) : "scc"); }
template <int OFF, bool SC1> DI void glds(unsigned voff, const void* sbase) {
  if (SC1) asm volatile("global_load_lds_dwordx4 %0, %1 offset:%2 sc1" ::"v"(voff), "s"(sbase), "i"(OFF) : "memory");
  else asm volatile("global_load_lds_dwordx4 %0, %1 offset:%2" ::"v"(voff), "s"(sbase), "i"(OFF) : "memory");
}
DI void touch_line(unsigned voff, const void* sbase) {
  asm volatile("global_load_lds_dword %0, %1" ::"v"(voff), "s"(sbase) : "memory");
}
template <int OFF> DI u32x4 ld16_raw(unsigned voff, const void* sbase) {
  u32x4 r; asm volatile("global_load_dwordx4 %0, %1, %2 offset:%3" : "=v"(r) : "v"(voff), "s"(sbase), "i"(OFF) : "memory"); return r;
}
template <int OFF> DI u32x2 ld8_raw(unsigned voff, const void* sbase) {
  u32x2 r; asm volatile("global_load_dwordx2 %0, %1, %2 offset:%3" : "=v"(r) : "v"(voff), "s"(sbase), "i"(OFF) : "memory"); return r;
}
template <int N, class T> DI void wait_v2(T& a, T& b) { asm volatile("s_waitcnt vmcnt(%2)" : "+v"(a), "+v"(b) : "n"(N) : "memory"); }
template <int N0, int N1, class T> DI void wait_v2s(int sel, T& a, T& b) {
  asm volatile("s_cmp_lg_u32 %2, 0\n\ts_cbranch_scc1 1f\n\ts_waitcnt vmcnt(%3)\n1:\n\ts_waitcnt vmcnt(%4)" : "+v"(a), "+v"(b) : "s"(sel), "n"(N0), "n"(N1) : "memory", "scc");
}
DI void lds_spin(LAS unsigned* w, unsigned want) {
  for (int sp = 0; sp < (1 << 24); ++sp) {
    if ((unsigned)__builtin_amdgcn_readfirstlane(*(volatile LAS unsigned*)w) >= want) break;
    __builtin_amdgcn_s_sleep(1);
  }
  FENCE();
}
DI void lds_arrive(LAS unsigned* w) { if ((threadIdx.x & 63) == 0) __hip_atomic_fetch_add(w, 1u, __ATOMIC_RELAXED, __HIP_MEMORY_SCOPE_WORKGROUP); }
DI rsrc_t mkrs(const void* p) { return __builtin_amdgcn_make_buffer_rsrc((void*)p, 0, 0x7fffffff, 0x00020000); }
DI void st16_dev(rsrc_t r, unsigned off, u32x4 v) { __builtin_amdgcn_raw_buffer_store_b128(v, r, off, 0, 16); }
DI void st8_dev(rsrc_t r, unsigned off, u32x2 v) { __builtin_amdgcn_raw_buffer_store_b64(v, r, off, 0, 16); }
DI u32x4 ld16_dev(rsrc_t r, unsigned off) { return __builtin_amdgcn_raw_buffer_load_b128(r, off, 0, 16); }
DI void signal(unsigned* c) { if ((threadIdx.x & 63) == 0) __hip_atomic_fetch_add(c, 1u, __ATOMIC_RELAXED, __HIP_MEMORY_SCOPE_AGENT); }
DI void wait_count(const unsigned* c, unsigned want) {
  for (int sp = 0; sp < (1 << 20); ++sp) {
    unsigned v;
    asm volatile("global_load_dword %0, %1, %2 sc1\n\ts_waitcnt vmcnt(0)" : "=v"(v) : "v"(0u), "s"(c) : "memory");
    if ((unsigned)__builtin_amdgcn_readfirstlane(v) >= want) break;
    __builtin_amdgcn_s_sleep(1);
  }
}
DI bool drained_last(LAS unsigned* cnt, unsigned n) {
  WAIT_V(0);
  unsigned old = 0;
  if ((threadIdx.x & 63) == 0) old = __hip_atomic_fetch_add(cnt, 1u, __ATOMIC_RELAXED, __HIP_MEMORY_SCOPE_WORKGROUP);
  return (unsigned)__builtin_amdgcn_readfirstlane(old) == n - 1;
}

#define ST_PARAM
#define ST_ARG(k)
#define ST_DECL
#define ST_AT(i)
#define ST_FLUSH()

template <int CTRL> DI float dpp_add(float v) {
  return v + __uint_as_float(__builtin_amdgcn_update_dpp(0u, __float_as_uint(v), CTRL, 0xf, 0xf, true));
}
DI void ln_apply(f32x2 (&v)[8]) {
  const float x0 = __uint_as_float(__builtin_amdgcn_readfirstlane(__float_as_uint(v[0].x)));
#pragma unroll
  for (int i = 0; i < 8; ++i) v[i] = v[i] - x0;
  const f32x2 s2 = ((v[0] + v[1]) + (v[2] + v[3])) + ((v[4] + v[5]) + (v[6] + v[7]));
  const f32x2 qa = v[0] * v[0] + v[1] * v[1], qb = v[2] * v[2] + v[3] * v[3], qc = v[4] * v[4] + v[5] * v[5], qd = v[6] * v[6] + v[7] * v[7];
  const f32x2 q2 = (qa + qb) + (qc + qd);
  const auto sw = __builtin_amdgcn_permlane16_swap(__float_as_uint(s2.x + s2.y), __float_as_uint(q2.x + q2.y), false, false);
  float c = __uint_as_float(sw[0]) + __uint_as_float(sw[1]);
  c = dpp_add<0xB1>(c); c = dpp_add<0x4E>(c); c = dpp_add<0x141>(c); c = dpp_add<0x140>(c);
  const float s = __uint_as_float(__builtin_amdgcn_readlane(__float_as_uint(c), 0)) + __uint_as_float(__builtin_amdgcn_readlane(__float_as_uint(c), 32));
  const float q = __uint_as_float(__builtin_amdgcn_readlane(__float_as_uint(c), 16)) + __uint_as_float(__builtin_amdgcn_readlane(__float_as_uint(c), 48));
  const float md = s * (1.0f / 1024.0f), var = fmaxf(q * (1.0f / 1024.0f) - md * md, 0.f);
  const float rstd = __builtin_amdgcn_rcpf(rbf(__builtin_amdgcn_sqrtf(rbf(rbf(var) + 1e-5f))));
  const float off = (x0 - rbf(x0 + md)) * rstd;
#pragma unroll
  for (int i = 0; i < 8; ++i) v[i] = v[i] * rstd + off;
}

struct RowC { u32x4 Fre, Fim, C0, S0, nS0, C1, nS1, nC1; f32x4 ta[2], tb[2], tc[2], td[2]; };
DI void rowfft_consts(RowC& k, int li, int g) {
  const bool sp = li == 0;
  float c[8], s[8];
#pragma unroll
  for (int j = 0; j < 8; ++j) {
    const int n1 = 4 * j + g; const float a = (float)((n1 * li) & 31) * (1.0f / 32.0f);
    c[j] = cosr(a); s[j] = sp ? ((n1 & 1) ? -1.0f : 1.0f) : -sinr(a);
  }
#pragma unroll
  for (int j = 0; j < 4; ++j) { k.Fre[j] = pk(c[2 * j], c[2 * j + 1]); k.Fim[j] = pk(s[2 * j], s[2 * j + 1]); }
#pragma unroll
  for (int j = 0; j < 8; ++j) { const float a = (float)(((8 * g + j) * li) & 31) * (1.0f / 32.0f); c[j] = cosr(a); s[j] = sinr(a); }
#pragma unroll
  for (int j = 0; j < 4; ++j) { k.C0[j] = pk(c[2 * j], c[2 * j + 1]); k.S0[j] = pk(s[2 * j], s[2 * j + 1]); k.nS0[j] = k.S0[j] ^ 0x80008000u; }
#pragma unroll
  for (int j = 0; j < 8; ++j) { const float a = (float)(((8 * g + j) * (li + 1)) & 31) * (1.0f / 32.0f); c[j] = cosr(a); s[j] = -sinr(a); }
#pragma unroll
  for (int j = 0; j < 4; ++j) { k.C1[j] = pk(c[2 * j], c[2 * j + 1]); k.nS1[j] = pk(s[2 * j], s[2 * j + 1]); k.nC1[j] = k.C1[j] ^ 0x80008000u; }
  const int k1e = sp ? 16 : li;
#pragma unroll
  for (int j = 0; j < 8; ++j) {
    const float a = (float)((8 * g + j) * k1e) * (1.0f / 1024.0f), cc = cosr(a), ss = sinr(a);
    k.ta[j & 1][j >> 1] = sp ? 0.f : cc; k.tb[j & 1][j >> 1] = sp ? cc : ss; k.tc[j & 1][j >> 1] = sp ? 0.f : -ss; k.td[j & 1][j >> 1] = sp ? -ss : cc;
  }
}
DI void pin_consts(RowC& k) {
  pin(k.Fre); pin(k.Fim); pin(k.C0); pin(k.S0); pin(k.nS0); pin(k.C1); pin(k.nS1); pin(k.nC1);
#pragma unroll
  for (int j = 0; j < 2; ++j) { pin(k.ta[j]); pin(k.tb[j]); pin(k.tc[j]); pin(k.td[j]); }
}
DI void rowfft_run(f32x2 (&v)[8], const RowC& k, LAS unsigned* L, unsigned* z) {
  const int tid = threadIdx.x, wid = tid >> 6, lane = tid & 63;
  const bool sp = (lane & 15) == 0;
  ln_apply(v);
  u32x4 A0, A1;
#pragma unroll
  for (int j = 0; j < 4; ++j) { A0[j] = pk(v[2 * j].x, v[2 * j + 1].x); A1[j] = pk(v[2 * j].y, v[2 * j + 1].y); }
  const f32x4 zero = {0.f, 0.f, 0.f, 0.f};
  const f32x4 r0 = mfma(A0, k.Fre, zero), i0 = mfma(A0, k.Fim, zero), r1 = mfma(A1, k.Fre, zero), i1 = mfma(A1, k.Fim, zero);
  const u32x4 Bre1 = zip4(mad2(k.ta[0], r0, k.tb[0], i0), mad2(k.ta[1], r1, k.tb[1], i1));
  const u32x4 Bim1 = zip4(mad2(k.tc[0], r0, k.td[0], i0), mad2(k.tc[1], r1, k.td[1], i1));
  const u32x4 Braw = zip4(r0, r1);
  u32x4 Bre0, Bim0;
#pragma unroll
  for (int r = 0; r < 4; ++r) { Bre0[r] = sp ? Braw[r] : Bre1[r]; Bim0[r] = sp ? 0u : Bim1[r]; }
  f32x4 xr0 = mfma(k.C0, Bre0, zero), xi0 = mfma(k.nS0, Bre0, zero), xr1 = mfma(k.C1, Bre1, zero), xi1 = mfma(k.nS1, Bre1, zero);
  const f32x4 ny = mfma(k.C1, Bre0, zero);
  xr0 = mfma(k.S0, Bim0, xr0); xi0 = mfma(k.C0, Bim0, xi0); xr1 = mfma(k.nS1, Bim1, xr1); xi1 = mfma(k.nC1, Bim1, xi1);
  const float nyq = __uint_as_float(__builtin_amdgcn_readlane(__float_as_uint(ny[3]), 48));
  xi0[0] = lane == 0 ? nyq : xi0[0];
  const u32x4 o0 = zip4(xr0, xi0), o1 = zip4(xr1, xi1);
  *(LAS u32x4*)(L + wid * 516 + lane * 4) = o0;
  *(LAS u32x4*)(L + wid * 516 + 256 + lane * 4) = o1;
  LBAR();
#pragma unroll
  for (int i = 0; i < 2; ++i) {
    const int ch = tid + 512 * i, sg = ch >> 1, jh = ch & 1;
    u32x4 o;
#pragma unroll
    for (int e = 0; e < 4; ++e) o[e] = L[(4 * jh + e) * 516 + sg];
    *(u32x4*)(z + ch * 4) = o;
  }
}

constexpr int T3_FRAGS = 15;
constexpr int T2_FRAGS = 20;
DI unsigned table_entry(int idx) {
  const int fr = idx >> 8, lane = (idx >> 2) & 63, e = idx & 3, li = lane & 15, g = lane >> 4;
  if (fr < T3_FRAGS) {
    const int u = fr & 1, n2 = 16 * u + li, r = 4 * g + e;
    if (fr < 10) {
      const int rr = (fr >= 4 && fr < 8) ? r + 1 : r;
      const float a = (float)((n2 * rr) & 31) * (1.0f / 32.0f), c = cosr(a), s = sinr(a);
      if (fr < 2) return pk(c, -s);
      if (fr < 4) return pk(s, c);
      if (fr < 6) return pk(c, -s);
      if (fr < 8) return pk(-s, -c);
      return r == 0 ? pk(1.0f, (li & 1) ? -1.0f : 1.0f) : pk(2.0f * c, -2.0f * s);
    }
    if (fr < 14) { const float a = (float)(n2 * r) * (1.0f / 1024.0f); return __float_as_uint(fr < 12 ? cosr(a) : sinr(a)); }
    const float a = (float)(16 * (e & 1) + li) * (1.0f / 64.0f);
    return __float_as_uint(2.0f * (e < 2 ? cosr(a) : sinr(a)));
  }
  const int f2 = fr - T3_FRAGS;
  if (f2 < 16) {
    const float a = (float)(((16 * (f2 & 3) + 4 * g + e) * (16 * (f2 >> 2) + li)) & 63) * (1.0f / 64.0f);
    return pk(cosr(a), sinr(a));
  }
  const int t = f2 & 1, s0 = 16 * (e >> 1) + 4 * g + 2 * (e & 1);
  const float a0 = (float)((s0 * (16 * t + li)) & 31) * (1.0f / 32.0f), a1 = (float)(((s0 + 1) * (16 * t + li)) & 31) * (1.0f / 32.0f);
  return f2 < 18 ? pk(cosr(a0), cosr(a1)) : pk(sinr(a0), sinr(a1));
}

struct FirstArgs { const u16* x; const u16* wr; const u16* wi; unsigned* z; unsigned* wt; unsigned* wq; unsigned* tab; unsigned* cnt; const u16* w1; };
constexpr int CNT_WORDS = 2 * 96 * 32;

__global__ __launch_bounds__(512) void k_first(FirstArgs p ST_PARAM) {
  __shared__ __attribute__((aligned(16))) char smem[64 * 68 * 4 + 256];
  ST_DECL; ST_AT(0);
  const int tid = threadIdx.x, wid = tid >> 6, lane = tid & 63, b = blockIdx.x;
  auto touch_w1 = [&]() {
    if (tid < 112) {
      const int r = (tid * 2341) >> 15;
      set_m0((unsigned)(size_t)(LAS char*)smem, 64 * 68 * 4);
      touch_line(((((b >> 2) & 1) * 512 + (b >> 3) * 8 + r) * F_ + (b & 3) * 896) * 2 + (tid - r * 14) * 128, p.w1);
    }
  };
  if (b >= 256) {
    if (b < 512) {
      const int blk = b - 256, kb = blk & 7, fg = blk >> 3, w = fg & 3, tg = fg >> 2;
      unsigned* L = (unsigned*)smem;
      const int k = 64 * kb + lane;
      if (wid == 7) {
        const int idx = blk * 8 + (lane >> 3), q = lane & 7;
        const int ln = (idx >> 2) & 63, f = 16 * (idx >> 9) + (ln & 15) + 64 * (16 * ((idx >> 8) & 1) + 4 * (ln >> 4) + (idx & 3));
        const float e = bflo(((q & 2) ? p.wi : p.wr)[((q & 1) ? (2048 - f) & 2047 : f) * 513 + (q & 4) * 128]);
        float h = (q & 3) == 3 ? -e : e;
        h = dpp_add<0xB1>(h);
        const float o = __uint_as_float(__builtin_amdgcn_update_dpp(0u, __float_as_uint(h), 0x141, 0xf, 0xf, true));
        const float im0 = __uint_as_float(__builtin_amdgcn_update_dpp(0u, __float_as_uint(h), 0x4E, 0xf, 0xf, true));
        const float re1 = __uint_as_float(__builtin_amdgcn_update_dpp(0u, __float_as_uint(o), 0x4E, 0xf, 0xf, true));
        const float sc = 4.76837158203125e-07f * 0.25f;
        if (q == 0) { p.wt[idx] = pk((h + re1) * sc, (im0 + o) * sc); p.wq[idx] = pk((h - re1) * sc, (im0 - o) * sc); }
      }
      unsigned short r[8], im[8];
#pragma unroll
      for (int i = 0; i < 8; ++i) {
        const int fi = wid * 8 + i, f = 16 * w + (fi >> 2) + 64 * (4 * tg + (fi & 3));
        r[i] = p.wr[f * 513 + k]; im[i] = p.wi[f * 513 + k];
      }
      const float sc = 4.76837158203125e-07f;
#pragma unroll
      for (int i = 0; i < 8; ++i) L[lane * 68 + wid * 8 + i] = pk(bflo(r[i]) * sc, bflo(im[i]) * sc);
      FENCE(); touch_w1();
      LBAR();
#pragma unroll
      for (int i = 0; i < 2; ++i) {
        const int idx = tid + 512 * i, kl = idx >> 4, f1l = idx & 15, kk = 64 * kb + kl;
        const int k1 = kk & 31, k2 = kk >> 5;
        const int t = k1 >= 16, l1 = (k1 <= 16) ? (k1 & 15) : 32 - k1;
        const int sigma = t * 256 + (l1 + 16 * (k2 >> 2)) * 4 + (k2 & 3);
        const u32x4 v = *(const u32x4*)(L + kl * 68 + f1l * 4);
        if (kk != 0) *(u32x4*)(p.wt + ((sigma * 4 + w) * 2 + (tg >> 2)) * 256 + ((tg & 3) * 16 + f1l) * 4) = v;
      }
    } else {
      const int idx = (b - 512) * 512 + tid;
      if (idx < (T3_FRAGS + T2_FRAGS) * 256) p.tab[idx] = table_entry(idx);
      if (idx < CNT_WORDS) p.cnt[idx] = 0u;
    }
    WAIT_V(0);
    ST_FLUSH();
    return;
  }
  const int s = 256 * (b >> 5) + 32 * wid + (b & 31);
  const unsigned* xr = (const unsigned*)(p.x + (size_t)s * D_);
  unsigned u[8];
#pragma unroll
  for (int j = 0; j < 8; ++j) u[j] = xr[64 * j + lane];
  RowC k;
  rowfft_consts(k, lane & 15, lane >> 4);
  ST_AT(1);
  f32x2 v[8];
#pragma unroll
  for (int j = 0; j < 8; ++j) { v[j] = (f32x2){bflo(u[j]), bfhi(u[j])}; pin(v[j]); }
  touch_w1();
  rowfft_run(v, k, (LAS unsigned*)smem, p.z + (size_t)b * 4096);
  WAIT_V(0);
  ST_AT(2); ST_FLUSH();
}
constexpr int FIRST_BLOCKS = 512 + ((T3_FRAGS + T2_FRAGS) * 256 + 511) / 512;
static_assert(CNT_WORDS <= (FIRST_BLOCKS - 512) * 512, "counter reset");

__global__ __launch_bounds__(512) void k_col(const unsigned* __restrict__ zin, unsigned* __restrict__ zout, const unsigned* __restrict__ wt,
                                             const unsigned* __restrict__ wq, const unsigned* __restrict__ tab ST_PARAM) {
  __shared__ __attribute__((aligned(16))) char smem_[32768 + T2_FRAGS * 1024];
  LAS char* smem = (LAS char*)smem_;
  ST_DECL; ST_AT(0);
  const int tid = threadIdx.x, wid = __builtin_amdgcn_readfirstlane(tid >> 6), lane = tid & 63, li = lane & 15, g = lane >> 4;
  const int pb = (blockIdx.x & 7) * 32 + (blockIdx.x >> 3), c = wid >> 2, w = wid & 3, sigma = 2 * pb + c;
  {
    const unsigned lb = (unsigned)(size_t)smem;
    const int P = wid * 128 + lane, beta = P >> 2, ch = (P & 3) ^ ((0x78 >> (2 * ((beta >> 2) & 3))) & 3);
    const unsigned vo = beta * 16384 + ch * 16;
    set_m0(lb + wid * 2048, 0);
    glds<0, false>(vo, zin + pb * 16);
    glds<1024, false>(vo + 16 * 16384 - 1024, zin + pb * 16);
    set_m0(lb + wid * 2048, 32768);
    glds<0, false>(lane * 16, tab + T3_FRAGS * 256 + wid * 512);
    glds<1024, false>(lane * 16, tab + T3_FRAGS * 256 + wid * 512);
    if (wid < 4) { set_m0(lb + wid * 1024, 32768 + 16384); glds<0, false>(lane * 16, tab + (T3_FRAGS + 16) * 256 + wid * 256); }
  }
  u32x4 Wv[2], Qv[2];
  Wv[0] = ld16_raw<0>(lane * 16, wt + (sigma * 4 + w) * 512);
  Wv[1] = ld16_raw<1024>(lane * 16, wt + (sigma * 4 + w) * 512);
  f32x4 t1c[2], t1s[2], t2c[2], t2s[2];
#pragma unroll
  for (int j = 0; j < 8; ++j) {
    const float a = (float)((16 * (j >> 2) + 4 * g + (j & 3)) * (16 * w + li)) * (1.0f / 2048.0f);
    t1c[j >> 2][j & 3] = cosr(a); t1s[j >> 2][j & 3] = sinr(a);
    const float b = (float)((16 * (j >> 2) + li) * (16 * w + 4 * g + (j & 3))) * (1.0f / 2048.0f);
    t2c[j >> 2][j & 3] = cosr(b); t2s[j >> 2][j & 3] = sinr(b);
  }
#pragma unroll
  for (int j = 0; j < 2; ++j) { pin(t1c[j]); pin(t1s[j]); pin(t2c[j]); pin(t2s[j]); }
  ST_AT(1);
  WAIT_V(2);
  BAR();
  ST_AT(2);
  u32x4 Ere[4], Eim[4], Gc[2], Gs[2], Gn[2], Z[4][2];
  {
    const LAS char* base = smem + (32 * (g >> 1) + li) * 64 + (((2 * c + (g & 1)) ^ ((0x78 >> (2 * (li >> 2))) & 3)) << 4);
#pragma unroll
    for (int k = 0; k < 4; ++k) {
      Ere[k] = *(const LAS u32x4*)(smem + 32768 + (w * 4 + k) * 1024 + lane * 16);
      Z[k][0] = *(const LAS u32x4*)(base + k * 4096); Z[k][1] = *(const LAS u32x4*)(base + k * 4096 + 1024);
    }
#pragma unroll
    for (int t = 0; t < 2; ++t) {
      Gc[t] = *(const LAS u32x4*)(smem + 32768 + (16 + t) * 1024 + lane * 16);
      Gs[t] = *(const LAS u32x4*)(smem + 32768 + (18 + t) * 1024 + lane * 16);
    }
  }
  SCHED();
  const f32x4 zero = {0.f, 0.f, 0.f, 0.f};
  f32x4 dr[2] = {zero, zero}, di[2] = {zero, zero};
#pragma unroll
  for (int k = 0; k < 4; ++k) {
    dr[0] = mfma(Z[k][0], Ere[k], dr[0]); dr[1] = mfma(Z[k][1], Ere[k], dr[1]);
#pragma unroll
    for (int e = 0; e < 4; ++e) Eim[k][e] = __builtin_amdgcn_alignbit(Ere[k][e], Ere[k][e], 16) ^ 0x8000u;
    di[0] = mfma(Z[k][0], Eim[k], di[0]); di[1] = mfma(Z[k][1], Eim[k], di[1]);
  }
  Gn[0] = Gs[0] ^ 0x80008000u; Gn[1] = Gs[1] ^ 0x80008000u;
  const u32x4 Bre = cat4(mad2(dr[0], t1c[0], di[0], t1s[0]), mad2(dr[1], t1c[1], di[1], t1s[1]));
  const u32x4 Bim = cat4(mad2(di[0], t1c[0], -dr[0], t1s[0]), mad2(di[1], t1c[1], -dr[1], t1s[1]));
  f32x4 xr[2], xi[2];
#pragma unroll
  for (int t = 0; t < 2; ++t) { xr[t] = mfma(Gc[t], Bre, zero); xi[t] = mfma(Gn[t], Bre, zero); }
#pragma unroll
  for (int t = 0; t < 2; ++t) { xr[t] = mfma(Gs[t], Bim, xr[t]); xi[t] = mfma(Gc[t], Bim, xi[t]); }
  f32x4 ur[2], ui[2];
  wait_v2<0>(Wv[0], Wv[1]);
#pragma unroll
  for (int t = 0; t < 2; ++t) {
    const f32x4 a = __builtin_bit_cast(f32x4, Wv[t] << 16), b = __builtin_bit_cast(f32x4, Wv[t] & 0xffff0000u);
    ur[t] = mad2(xr[t], a, -xi[t], b); ui[t] = mad2(xr[t], b, xi[t], a);
  }
  if (pb == 0) {
    Qv[0] = *(const u32x4*)(wq + (w * 2 + 0) * 256 + lane * 4);
    Qv[1] = *(const u32x4*)(wq + (w * 2 + 1) * 256 + lane * 4);
    LAS f32x2* X = (LAS f32x2*)smem;
    LBAR();
    if (c == 0) {
#pragma unroll
      for (int t = 0; t < 2; ++t)
#pragma unroll
        for (int r = 0; r < 4; ++r) X[16 * w + li + 64 * (16 * t + 4 * g + r)] = (f32x2){xr[t][r], xi[t][r]};
    }
    LBAR();
    if (c == 0) {
#pragma unroll
      for (int t = 0; t < 2; ++t)
#pragma unroll
        for (int r = 0; r < 4; ++r) {
          const f32x2 m = X[(2048 - (16 * w + li + 64 * (16 * t + 4 * g + r))) & 2047];
          const float a = bflo(Qv[t][r]), b = bfhi(Qv[t][r]);
          ur[t][r] += m.x * a + m.y * b; ui[t][r] += m.x * b - m.y * a;
        }
    }
  }
  const u32x4 Are = cat4(ur[0], ur[1]), Aim = cat4(ui[0], ui[1]);
  LAS char* ex = smem + 16384 + c * 8192;
  {
    f32x4 tr[2], ti[2];
#pragma unroll
    for (int t = 0; t < 2; ++t) { tr[t] = mfma(Are, Gc[t], zero); ti[t] = mfma(Are, Gs[t], zero); }
#pragma unroll
    for (int t = 0; t < 2; ++t) { tr[t] = mfma(Aim, Gn[t], tr[t]); ti[t] = mfma(Aim, Gc[t], ti[t]); }
#pragma unroll
    for (int t = 0; t < 2; ++t)
      *(LAS u32x4*)(ex + (w * 2 + t) * 1024 + lane * 16) = zip4(mad2(tr[t], t2c[t], -ti[t], t2s[t]), mad2(-tr[t], t2s[t], -ti[t], t2c[t]));
  }
  ST_AT(3);
  LBAR();
  ST_AT(4);
  {
#pragma unroll
    for (int k = 0; k < 4; ++k) { Z[k][0] = *(const LAS u32x4*)(ex + (k * 2) * 1024 + lane * 16); Z[k][1] = *(const LAS u32x4*)(ex + (k * 2 + 1) * 1024 + lane * 16); }
    f32x4 orr[2] = {zero, zero}, oi[2] = {zero, zero};
#pragma unroll
    for (int k = 0; k < 4; ++k)
#pragma unroll
      for (int t = 0; t < 2; ++t) { orr[t] = mfma(Ere[k], Z[k][t], orr[t]); oi[t] = mfma(Eim[k], Z[k][t], oi[t]); }
#pragma unroll
    for (int t = 0; t < 2; ++t) {
      const int beta = 32 * (2 * w + (g >> 1)) + 16 * t + li;
      *(u32x4*)(zout + (size_t)beta * 4096 + pb * 16 + (2 * c + (g & 1)) * 4) = zip4(orr[t], -oi[t]);
    }
  }
  ST_AT(5); ST_FLUSH();
}

template <int BN, int KT, int NS, int A0, int ASTR, int B0, int BSTR>
struct Mma {
  static constexpr int MB = 4, NT = BN / 32, NM = MB * NT, PITCH = BN * 2, NR = MB + 2 * NT;
  static constexpr int NBB = BN == 224 ? 2 : NT;
  static constexpr int SPB = (NS * BSTR <= 65536) ? NS : 2, NBS = (NS + SPB - 1) / SPB;
  static DI int swz(int k) { return BN == 224 ? ((k >> 2) & 1) << 1 : (k & 7) << 1; }
  template <int DRAIN_AT, class INIT, class HOOK>
  static DI void run(LAS char* smem, f32x4 (&acc)[NT][MB], int wm, int wn, INIT&& init, HOOK&& hook) {
    const int lane = threadIdx.x & 63, g = lane >> 4;
    const LAS char *pa, *pb[NBS][NBB];
    pa = smem + A0 + wm * MB * 2048 + lane * 16; pin(pa);
    {
      const int q = (lane >> 2) & 3, pp = lane & 3, r = 4 * g + q;
#pragma unroll
      for (int s = 0; s < NBS; ++s)
#pragma unroll
        for (int n = 0; n < NBB; ++n) { pb[s][n] = smem + B0 + s * SPB * BSTR + r * PITCH + (((2 * (wn * NT + n) + (pp >> 1)) ^ swz(r)) << 4) + 8 * (pp & 1); pin(pb[s][n]); }
    }
    u32x4 Fa[2][MB]; u32x2 Fb[2][NT][2];
    auto rd1 = [&](auto SET, auto ST, auto KS, auto J) {
      constexpr int set = SET, st = ST, ks = KS, j = J;
      if constexpr (j >= 2 && j < 2 + MB) Fa[set][j - 2] = *(const LAS u32x4*)(pa + st * ASTR + (j - 2) * 2048 + ks * 1024);
      else {
        constexpr int jj = j < 2 ? j : j - MB, n = jj >> 1, h = jj & 1;
        constexpr int nb = NBB == NT ? n : (n & 1), extra = NBB == NT ? 0 : (n & ~1) * 32;
        Fb[set][n][h] = tr_read(pb[st / SPB][nb] + (st % SPB) * BSTR + extra + ks * 32 * PITCH + h * 16 * PITCH);
      }
    };
    auto mm1 = [&](auto SET, auto N, auto M) {
      constexpr int set = SET, n = N, m = M;
      const u32x4 b = {Fb[set][n][0].x, Fb[set][n][0].y, Fb[set][n][1].x, Fb[set][n][1].y};
      acc[n][m] = mfma(b, Fa[set][m], acc[n][m]);
    };
    auto phase = [&](auto SET, auto ST, auto KS) {
      sfor<0, NM>([&](auto I) {
        constexpr int i = I;
        if constexpr (i == NM - 1) { WAIT_L0(); SCHED(); }
        mm1(SET, IC<i / MB>{}, IC<i % MB>{});
        SCHED();
        if constexpr (i < NR) rd1(IC<1 - SET>{}, ST, KS, I);
        SCHED();
      });
    };
    auto tile = [&](auto S, auto DR) {
      using NX = IC<(S + 1) % NS>;
      if constexpr (DR) WAIT_V(0);
      BAR();
      SCHED();
      phase(IC<1>{}, NX{}, IC<0>{});
      phase(IC<0>{}, NX{}, IC<1>{});
    };
    BAR();
    sfor<0, NR>([&](auto J) { rd1(IC<0>{}, IC<0>{}, IC<0>{}, J); });
    init();
    SCHED();
    phase(IC<0>{}, IC<0>{}, IC<1>{});
    for (int t = 0; t < (KT - 1) / NS; ++t) { hook(t); SCHED(); sfor<0, NS>([&](auto S) { tile(S, std::bool_constant<S == DRAIN_AT>{}); }); }
    sfor<0, (KT - 1) % NS>([&](auto S) { tile(S, std::false_type{}); });
    BAR();
    SCHED();
    sfor<0, NM>([&](auto I) { mm1(IC<1>{}, IC<I % NT>{}, IC<I / NT>{}); });
    SCHED();
  }
};

constexpr int L_A = 0, L_G1B = 49152, L_G2B = 65536, L_BIAS = 135168, L_CNT = L_BIAS + 512, L_JUNK = L_BIAS + 768, L_X1 = 136192, L_TOTAL = L_X1 + 16384;
constexpr int L_ZT = 0, L_XT = 16512, L_TAB = 32896;
typedef Mma<224, 16, 3, L_A, 16384, L_G1B, 28672> M1;
typedef Mma<128, 28, 4, L_A, 16384, L_G2B, 16384> M2;

struct BlockArgs {
  const unsigned* zin; const u16* xin; const unsigned* tab;
  const u16* w1; const u16* b1; const u16* w2; const u16* b2;
  u16* xn; u16* h; u16* part; unsigned* cnt;
  u16* xout;
  unsigned* zout; const unsigned* wt;
};

template <bool FIRST, bool LAST>
__global__ __launch_bounds__(512) void k_block(BlockArgs p ST_PARAM) {
  extern __shared__ __attribute__((aligned(16))) char dsm[];
  LAS char* smem = (LAS char*)dsm;
  ST_DECL; ST_AT(0);
  const int tid = threadIdx.x, wid = __builtin_amdgcn_readfirstlane(tid >> 6), lane = tid & 63, li = lane & 15, g = lane >> 4;
  const int bx = blockIdx.x & 7, bi = blockIdx.x >> 3;
  const int mt = (bx >> 2) * 8 + (bi & 7), jt = (bx & 3) * 4 + (bi >> 3);
  const int beta = mt * 16 + jt;
  const unsigned lds0 = (unsigned)(size_t)smem;
  LAS unsigned* lcnt = (LAS unsigned*)(smem + L_CNT);
  unsigned* c_xn = p.cnt + (mt * 6 + 0) * 32; unsigned* c_p = p.cnt + (mt * 6 + 1) * 32;
  unsigned* c_he = p.cnt + (mt * 6 + 2 + (jt >> 3)) * 32; unsigned* c_hl = c_he + 64;

  const int lw = wid & 3;
  const char* g1B = (const char*)p.w1 + ((size_t)(lw * 16) * F_ + jt * 224) * 2 - 4096;
  auto g1_off = [&](unsigned (&vb)[7]) {
    int l2 = lane; pin(l2);
    int r = (l2 * 37) >> 10, c = l2 - r * 28;
#pragma unroll
    for (int i = 0; i < 7; ++i) {
      vb[i] = r * (F_ * 2) + ((c ^ M1::swz(r)) << 4) + 4096 - (i & 3) * 1024;
      r += 2; c += 8; if (c >= 28) { c -= 28; r += 1; }
    }
  };
  auto g1_tB = [&](const unsigned (&vb)[7], int s) {
    sfor<0, 7>([&](auto I) { constexpr int i = I;
      if constexpr ((i & 3) == 0) set_m0(lds0 + L_G1B + lw * 7168 + s * 28672, i * 1024);
      glds<(i & 3) * 1024, false>(vb[i], g1B); });
    g1B += (size_t)64 * F_ * 2;
  };

  {
    u32x4 zi[2];
    zi[0] = ld16_raw<0>(tid * 16, p.zin + (size_t)beta * 4096);
    zi[1] = ld16_raw<0>(tid * 16, p.zin + (size_t)beta * 4096 + 2048);
    set_m0(lds0 + wid * 2048, L_TAB);
    glds<0, false>(lane * 16, p.tab + wid * 512);
    if (wid < 7) glds<1024, false>(lane * 16, p.tab + wid * 512);
    if (tid < 16) lcnt[tid] = 0u;
    unsigned vb[7];
    if (wid >= 4) {
      g1_off(vb);
      if (lw == 0) { set_m0(lds0, L_BIAS); if (lane < 28) glds<0, false>(lane * 16, p.b1 + jt * 224); }
      g1_tB(vb, 0);
    }
    wait_v2s<0, 7>(wid >> 2, zi[0], zi[1]);
    u32x4 res[2]; u32x2 rs[4];
    if (FIRST) {
      const u16* xr = p.xin + (size_t)(256 * (beta >> 5) + 32 * wid + (beta & 31)) * D_;
      rs[0] = ld8_raw<0>(li * 64 + g * 8, xr); rs[1] = ld8_raw<32>(li * 64 + g * 8, xr);
      rs[2] = ld8_raw<1024>(li * 64 + g * 8, xr); rs[3] = ld8_raw<1056>(li * 64 + g * 8, xr);
    } else {
      const u16* xr = p.xin + (size_t)(beta * 8 + wid) * D_;
      res[0] = ld16_raw<0>(li * 64 + g * 16, xr); res[1] = ld16_raw<1024>(li * 64 + g * 16, xr);
    }
    LAS unsigned* L = (LAS unsigned*)(smem + L_ZT);
#pragma unroll
    for (int i = 0; i < 2; ++i) {
      const int ch = tid + 512 * i, sg = ch >> 1, jh = ch & 1;
#pragma unroll
      for (int e = 0; e < 4; ++e) L[(4 * jh + e) * 516 + sg] = zi[i][e];
    }
    LBAR();
    ST_AT(1);
    const u32x4 X0 = *(const LAS u32x4*)(L + wid * 516 + lane * 4), X1 = *(const LAS u32x4*)(L + wid * 516 + 256 + lane * 4);
    u32x4 T[T3_FRAGS];
#pragma unroll
    for (int f = 0; f < T3_FRAGS; ++f) T[f] = *(const LAS u32x4*)(smem + L_TAB + f * 1024 + lane * 16);
    SCHED();
    if (wid >= 4) g1_tB(vb, 1);
    const unsigned dn = __builtin_amdgcn_readfirstlane(X0[0]);
    const float dc = bflo(dn), nyq = bfhi(dn);
    const f32x4 zero = {0.f, 0.f, 0.f, 0.f};
    const f32x4 hh = __builtin_bit_cast(f32x4, T[14]);
    f32x4 p0r[2], p0i[2], vr[2], vi[2];
#pragma unroll
    for (int u = 0; u < 2; ++u) { p0r[u] = mfma(X0, T[u], zero); p0i[u] = mfma(X0, T[2 + u], zero); }
#pragma unroll
    for (int u = 0; u < 2; ++u) { vr[u] = mfma(X1, T[4 + u], p0r[u]); vi[u] = mfma(X1, T[6 + u], p0i[u]); }
    const float dn2 = ((li & 1) ? -nyq : nyq) - dc;
    u32x4 A[2];
#pragma unroll
    for (int u = 0; u < 2; ++u) {
      const f32x4 twc = __builtin_bit_cast(f32x4, T[10 + u]), tws = __builtin_bit_cast(f32x4, T[12 + u]);
      f32x4 x = mad2(vr[u], twc, -vi[u], tws), y = mad2(vr[u], tws, vi[u], twc);
      const float sx = 2.0f * p0r[u][0] + dn2;
      const float sy = hh[u] * (vr[u][0] - p0r[u][0]) - hh[2 + u] * (vi[u][0] - p0i[u][0]);
      x[0] = g == 0 ? sx : x[0]; y[0] = g == 0 ? sy : y[0];
      A[u] = zip4(x, y);
    }
    if (FIRST) {
      wait_v2s<0, 7>(wid >> 2, rs[0], rs[1]); wait_v2<7>(rs[2], rs[3]);
      res[0] = (u32x4){rs[0].x, rs[0].y, rs[1].x, rs[1].y}; res[1] = (u32x4){rs[2].x, rs[2].y, rs[3].x, rs[3].y};
    } else {
      wait_v2s<0, 7>(wid >> 2, res[0], res[1]);
    }
    f32x2 v[8];
#pragma unroll
    for (int vv = 0; vv < 2; ++vv) {
      const u32x4 cb = T[8 + vv];
#pragma unroll
      for (int u = 0; u < 2; ++u) {
        const f32x4 r4 = {bflo(res[vv][2 * u]), bfhi(res[vv][2 * u]), bflo(res[vv][2 * u + 1]), bfhi(res[vv][2 * u + 1])};
        const f32x4 y = mfma(A[u], cb, r4);
        v[4 * vv + 2 * u] = (f32x2){y[0], y[1]}; v[4 * vv + 2 * u + 1] = (f32x2){y[2], y[3]};
      }
      const int n1 = 16 * vv + li;
      *(LAS u32x4*)(smem + L_X1 + n1 * 512 + (((wid * 4 + g) ^ (n1 & 7)) << 4)) = (u32x4){pk2(v[4 * vv]), pk2(v[4 * vv + 1]), pk2(v[4 * vv + 2]), pk2(v[4 * vv + 3])};
    }
    ln_apply(v);
#pragma unroll
    for (int vv = 0; vv < 2; ++vv) {
      const int n1 = 16 * vv + li;
      *(LAS u32x4*)(smem + L_XT + (((n1 * 4 + g) * 8) + (wid ^ (n1 & 7))) * 16) = (u32x4){pk2(v[4 * vv]), pk2(v[4 * vv + 1]), pk2(v[4 * vv + 2]), pk2(v[4 * vv + 3])};
    }
    LBAR();
    ST_AT(2);
  }

  RowC rc;
  if (wid >= 4) {
    {
      const char* gA = (const char*)p.xn + (size_t)(mt * 8 + lw * 2) * 32768;
      const unsigned lA = lds0 + L_A + lw * 4096;
      const unsigned va0 = lane * 16, va1 = lane * 16 + 32768 - 2048;
      auto tA = [&](int s) {
        set_m0(lA + s * 16384, 0);
        glds<0, true>(va0, gA); glds<1024, true>(va0, gA); glds<2048, true>(va1, gA); glds<3072, true>(va1, gA);
        gA += 2048;
      };
      unsigned vb[7];
      g1_off(vb);
      g1_tB(vb, 2);
      BAR();
      tA(0); tA(1); tA(2);
      WAIT_V(8); BAR();
      WAIT_V(4); BAR(); tA(0); g1_tB(vb, 0);
      WAIT_V(11); BAR(); tA(1); g1_tB(vb, 1);
      int cur = 2;
      for (int t = 2; t < 13; ++t) {
        WAIT_V(11); BAR(); tA(cur); g1_tB(vb, cur); cur = cur == 2 ? 0 : cur + 1;
        if (FIRST && t == 3 + 2 * lw) {
          set_m0(lds0, L_JUNK);
          if (lane < 56) touch_line((lw * 7 + (lane >> 3)) * 2048 + (lane & 7) * 128,
                                    (const char*)p.w2 + ((size_t)((jt >> 3) * 1792 + (bx >> 2) * 896 + bi * 28) * D_ + (bx & 1) * 512) * 2);
        }
      }
      WAIT_V(11); BAR();
      WAIT_V(0); BAR();
      BAR();
    }
    {
      int ktab;
      { const int e = lane - 7, l = lane - 35, l3 = (l * 43) >> 7, lr = l - 3 * l3;
        const int d = lane < 7 ? 0 : lane < 35 ? 1 + (e >> 2) : 1 + l3;
        const int o = lane < 7 ? lane : lane < 35 ? (e & 1) + (e & 2) * 2 : (lr == 2 ? 6 : 2 + lr);
        ktab = 7 * (d == 0 ? (jt & 7) : d - 1 + (d - 1 >= (jt & 7))) + o; }
      const char* gA = (const char*)p.h + ((size_t)(mt * 8 + (lw & 1) * 4) * 112 + (jt >> 3) * 56) * 1024;
      const char* gB = (const char*)p.w2 + ((size_t)((jt >> 3) * 1792 + (lw & 1) * 16) * D_ + (jt & 7) * 128) * 2;
      const unsigned lA = lds0 + L_A + (lw & 1) * 8192 + (lw >> 1) * 1024, lB = lds0 + L_G2B + lw * 4096;
      const unsigned va = lane * 16;
      unsigned vb0, vb1;
      { const int r = lane >> 4, c = lane & 15;
        vb0 = r * 2048 + ((c ^ M2::swz(r)) << 4); vb1 = (r + 4) * 2048 + ((c ^ M2::swz(r + 4)) << 4) - 1024; }
      auto tA = [&](int t) {
        const char* a = gA + __builtin_amdgcn_readlane(ktab, 2 * t + (lw >> 1)) * 1024;
        sfor<0, 4>([&](auto I) { constexpr int i = I; set_m0(lA + (t & 3) * 16384, i * 2048); glds<0, true>(va, a + i * 112 * 1024); });
      };
      auto tB = [&](int t) {
        const char* b = gB + (size_t)__builtin_amdgcn_readlane(ktab, 2 * t + (lw >> 1)) * 65536;
        set_m0(lB + (t & 3) * 16384, 0);
        glds<0, false>(vb0, b); glds<1024, false>(vb1, b); glds<2048, false>(vb0 + 16384 - 2048, b); glds<3072, false>(vb1 + 16384 - 2048, b);
      };
      tB(0); tB(1); tB(2); tB(3);
      {
        const rsrc_t rh = mkrs(p.h + ((size_t)(mt * 8 + lw * 2) * 112 + jt * 7) * 512);
        sfor<0, 4>([&](auto Q) { constexpr int q = Q;
          lds_spin(lcnt + 8 + q, 4);
          u32x4 o[4];
#pragma unroll
          for (int i = 0; i < (q == 3 ? 2 : 4); ++i) { const int kb = q + 4 * (i >> 1); o[i] = *(const LAS u32x4*)(smem + L_A + (kb >> 1) * 16384 + (lw * 2 + (i & 1)) * 2048 + (kb & 1) * 1024 + lane * 16); }
#pragma unroll
          for (int i = 0; i < (q == 3 ? 2 : 4); ++i) st16_dev(rh, ((i & 1) * 112 + q + 4 * (i >> 1)) * 1024 + lane * 16, o[i]);
          if (q == 2) {
            WAIT_V(4);
            unsigned old = 0;
            if (lane == 0) old = __hip_atomic_fetch_add(lcnt + 4, 1u, __ATOMIC_RELAXED, __HIP_MEMORY_SCOPE_WORKGROUP);
            if (__builtin_amdgcn_readfirstlane(old) == 3) signal(c_he);
          }
        });
      }
      BAR();
      BAR();
      if (lw == 0) { wait_count(c_he, 8); if (lane == 0) lcnt[3] = 1u; } else lds_spin(lcnt + 3, 1);
      if (lw >> 1) tA(3);
      tA(4); tB(4);
      BAR();
      tA(5); tB(5);
      WAIT_V(16); BAR();
      if (lw == 0) signal(c_hl);
      tA(6); tB(6);
      for (int t = 3; t < 10; ++t) { WAIT_V(16); BAR(); tA(t + 4); tB(t + 4); }
      unsigned late;
      WAIT_V(16); BAR();
      asm volatile("global_load_dword %0, %1, %2 sc1" : "=v"(late) : "v"(0u), "s"(c_hl) : "memory");
      tA(14); tB(14);
      WAIT_V(16); BAR(); tA(15); tB(15);
      asm volatile("s_waitcnt vmcnt(16)" : "+v"(late) :: "memory"); BAR();
      if ((unsigned)__builtin_amdgcn_readfirstlane(late) < 8u) wait_count(c_hl, 8);
      tA(16); tB(16);
      for (int t = 13; t < 24; ++t) { WAIT_V(16); BAR(); tA(t + 4); tB(t + 4); }
      WAIT_V(16); BAR();
      WAIT_V(8); BAR();
      WAIT_V(0); BAR();
      BAR();
    }
    if (!LAST) {
      set_m0(lds0, L_JUNK);
      if (lw < 2) touch_line((lw * 64 + lane) * 128, p.wt + (bx * 32 + bi) * 4096);
      int l2 = lane; pin(l2); rowfft_consts(rc, l2 & 15, l2 >> 4); pin_consts(rc);
    }
    if (lw == 0) wait_count(c_p, 16);
  } else {
    const int wm = wid & 1, wn = wid >> 1;
    {
      const rsrc_t rx = mkrs(p.xn + (size_t)(beta >> 1) * 16384);
#pragma unroll
      for (int i = 0; i < 4; ++i) {
        const int idx = tid + 256 * i, unit = idx >> 3, n1 = unit >> 2, j = (idx & 7) ^ (n1 & 7);
        st16_dev(rx, (unit * 16 + (beta & 1) * 8 + j) * 16, *(const LAS u32x4*)(smem + L_XT + idx * 16));
      }
      if (drained_last(lcnt + 0, 4)) { signal(c_xn); wait_count(c_xn, 16); }
      BAR();
    }
    ST_AT(3);
    {
      f32x4 acc[7][4];
      M1::run<-1>(smem, acc, wm, wn, [&]() {
#pragma unroll
        for (int n = 0; n < 7; ++n) {
          const u32x2 b = *(const LAS u32x2*)(smem + L_BIAS + (wn * 7 + n) * 32 + g * 8);
          const f32x4 bv = {bflo(b.x), bfhi(b.x), bflo(b.y), bfhi(b.y)};
#pragma unroll
          for (int m = 0; m < 4; ++m) acc[n][m] = bv;
        }
      }, [](int) {});
      ST_AT(4);
      auto gelu4 = [&](f32x4 a) -> u32x2 {
        const float k1 = -2.0f * 1.4426950408889634f * 0.7978845608028654f, k2 = k1 * 0.044715f;
        const f32x2 x0 = {a[0], a[1]}, x1 = {a[2], a[3]};
        f32x2 t0 = x0 * (x0 * x0 * k2 + k1), t1 = x1 * (x1 * x1 * k2 + k1);
        t0.x = __builtin_amdgcn_exp2f(t0.x); t0.y = __builtin_amdgcn_exp2f(t0.y); t1.x = __builtin_amdgcn_exp2f(t1.x); t1.y = __builtin_amdgcn_exp2f(t1.y);
        t0 = t0 + 1.0f; t1 = t1 + 1.0f;
        t0.x = __builtin_amdgcn_rcpf(t0.x); t0.y = __builtin_amdgcn_rcpf(t0.y); t1.x = __builtin_amdgcn_rcpf(t1.x); t1.y = __builtin_amdgcn_rcpf(t1.y);
        return (u32x2){pk2(t0 * x0), pk2(t1 * x1)};
      };
      auto epi = [&](auto WN) {
        constexpr int w = WN;
#pragma unroll
        for (int q = 0; q < 4; ++q) {
#pragma unroll
          for (int m = 0; m < 4; ++m) {
            const int kb = q == 3 ? 3 : w * 4 + q;
            LAS char* lo = smem + L_A + (kb >> 1) * 16384 + (wm * 4 + m) * 2048 + (kb & 1) * 1024 + lane * 16;
            if (q == 3) {
              const u32x2 o = gelu4(acc[w == 0 ? 6 : 0][m]);
              *(LAS u32x2*)(lo + w * 8) = o;
            } else {
              const int n = 2 * q + w;
              const u32x2 a = gelu4(acc[n][m]), b = gelu4(acc[n + 1][m]);
              const u32x4 o = {a.x, a.y, b.x, b.y};
              *(LAS u32x4*)lo = o;
            }
            SCHED();
            if (q > 0 && m == 0) { asm volatile("s_waitcnt lgkmcnt(1)" ::: "memory"); lds_arrive(lcnt + 7 + q); SCHED(); }
          }
        }
      };
      if (wn == 0) epi(IC<0>{}); else epi(IC<1>{});
      WAIT_L0(); FENCE(); lds_arrive(lcnt + 11);
      ST_AT(5);
    }
    {
      f32x4 acc[4][4];
      M2::run<-1>(smem, acc, wm, wn, [&]() {
#pragma unroll
        for (int n = 0; n < 4; ++n)
#pragma unroll
          for (int m = 0; m < 4; ++m) acc[n][m] = (f32x4){0.f, 0.f, 0.f, 0.f};
      }, [&](int it) {
      });
      ST_AT(8);
      const rsrc_t rp = mkrs(p.part + (size_t)(jt >> 3) * S_ * D_ + (size_t)(mt * 16 + wm * 8) * 8192);
#pragma unroll
      for (int m = 0; m < 4; ++m)
#pragma unroll
        for (int q = 0; q < 2; ++q) {
          const f32x4 a = acc[2 * q][m], b = acc[2 * q + 1][m];
          const u32x4 o = {pk(a[0], a[1]), pk(a[2], a[3]), pk(b[0], b[1]), pk(b[2], b[3])};
          st16_dev(rp, (2 * m + (li >> 3)) * 16384 + ((jt & 7) * 4 + wn * 2 + q) * 512 + (li & 7) * 64 + g * 16, o);
        }
      if (drained_last(lcnt + 1, 4)) signal(c_p);
      ST_AT(9);
      if (!LAST) { int l2 = lane; pin(l2); rowfft_consts(rc, l2 & 15, l2 >> 4); pin_consts(rc); }
    }
  }
  u32x2 bb[2][2];
#pragma unroll
  for (int i = 0; i < 2; ++i) {
    const int c = tid + 512 * i, blk = c >> 5, gg = ((c & 31) ^ (blk & 7)) & 3;
    bb[i][0] = *(const u32x2*)(p.b2 + 32 * blk + 4 * gg); bb[i][1] = *(const u32x2*)(p.b2 + 32 * blk + 16 + 4 * gg);
  }
  BAR();
  ST_AT(10);
  {
    const rsrc_t rp = mkrs(p.part + (size_t)beta * 8192);
    u32x4 a0[2], a1[2];
#pragma unroll
    for (int i = 0; i < 2; ++i) {
      const int c = tid + 512 * i, blk = c >> 5, slot = (c & 31) ^ (blk & 7);
      a0[i] = ld16_dev(rp, blk * 512 + slot * 16); a1[i] = ld16_dev(rp, blk * 512 + slot * 16 + S_ * D_ * 2);
    }
    LAS char* I = smem + 16640;
#pragma unroll
    for (int i = 0; i < 2; ++i) {
      const int c = tid + 512 * i, blk = c >> 5, slot = (c & 31) ^ (blk & 7), j = slot >> 2, gg = slot & 3;
      const u32x4 x1 = *(const LAS u32x4*)(smem + L_X1 + c * 16);
      u32x4 o;
#pragma unroll
      for (int e = 0; e < 4; ++e) {
        const unsigned b = bb[i][e >> 1][e & 1];
        o[e] = pk(bflo(x1[e]) + (bflo(a0[i][e]) + bflo(a1[i][e]) + bflo(b)), bfhi(x1[e]) + (bfhi(a0[i][e]) + bfhi(a1[i][e]) + bfhi(b)));
      }
      if (LAST) {
        *(LAS u32x2*)(I + j * 2112 + blk * 64 + gg * 8) = (u32x2){o[0], o[1]};
        *(LAS u32x2*)(I + j * 2112 + blk * 64 + 32 + gg * 8) = (u32x2){o[2], o[3]};
      } else {
        *(LAS u32x4*)(I + j * 2112 + blk * 64 + gg * 16) = o;
        *(u32x4*)(p.xout + (size_t)(beta * 8 + j) * D_ + blk * 32 + gg * 8) = o;
      }
    }
    LBAR();
    ST_AT(11);
    if (LAST) {
      const int s = 256 * (beta >> 5) + 32 * wid + (beta & 31);
#pragma unroll
      for (int i = 0; i < 2; ++i) *(u32x4*)(p.xout + (size_t)s * D_ + i * 512 + lane * 8) = *(const LAS u32x4*)(I + wid * 2112 + i * 1024 + lane * 16);
    } else {
      const int n2 = 2 * li, pos = 8 * ((n2 >> 2) & 3) + 4 * (n2 >> 4) + (n2 & 3);
      f32x2 v[8];
#pragma unroll
      for (int j = 0; j < 8; ++j) {
        const unsigned u = *(const LAS unsigned*)(I + wid * 2112 + (4 * j + g) * 64 + pos * 2);
        v[j] = (f32x2){bflo(u), bfhi(u)};
      }
      rowfft_run(v, rc, (LAS unsigned*)smem, p.zout + (size_t)beta * 4096);
    }
  }
  ST_AT(12); ST_FLUSH();
}

extern "C" void kernel_launch(void* const* d_in, const int* in_sizes, int n_in, void* d_out, int out_size, void* d_ws, size_t ws_size,
                              hipStream_t stream) {
  const u16* x = (const u16*)d_in[0];
  char* ws = (char*)d_ws;
  const size_t MB = 1u << 20;
  unsigned* wt = (unsigned*)(ws); unsigned* wq = (unsigned*)(ws + 4 * MB); unsigned* tab = (unsigned*)(ws + 4 * MB + 65536);
  unsigned* cnt = (unsigned*)(ws + 4 * MB + 131072);
  unsigned* z1 = (unsigned*)(ws + 5 * MB); unsigned* z2 = (unsigned*)(ws + 9 * MB);
  u16* x2 = (u16*)(ws + 17 * MB);
  (void)hipFuncSetAttribute((const void*)k_block<true, false>, hipFuncAttributeMaxDynamicSharedMemorySize, L_TOTAL);
  (void)hipFuncSetAttribute((const void*)k_block<false, true>, hipFuncAttributeMaxDynamicSharedMemorySize, L_TOTAL);
  FirstArgs f; f.x = x; f.wr = (const u16*)d_in[1]; f.wi = (const u16*)d_in[2]; f.z = z1; f.wt = wt; f.wq = wq; f.tab = tab; f.cnt = cnt; f.w1 = (const u16*)d_in[4];
  BlockArgs a; a.zin = z2; a.xin = x; a.tab = tab; a.w1 = (const u16*)d_in[4]; a.b1 = (const u16*)d_in[5]; a.w2 = (const u16*)d_in[6]; a.b2 = (const u16*)d_in[7];
  a.xn = (u16*)(ws + 25 * MB); a.h = (u16*)(ws + 29 * MB); a.part = (u16*)(ws + 44 * MB); a.cnt = cnt; a.xout = x2; a.zout = z1; a.wt = wt;
  k_first<<<FIRST_BLOCKS, 512, 0, stream>>>(f ST_ARG(0));
  k_col<<<256, 512, 0, stream>>>(z1, z2, wt, wq, tab ST_ARG(1));
  k_block<true, false><<<256, 512, L_TOTAL, stream>>>(a ST_ARG(2));
  k_col<<<256, 512, 0, stream>>>(z1, z2, wt, wq, tab ST_ARG(3));
  a.xin = x2; a.cnt = cnt + CNT_WORDS / 2; a.xout = (u16*)d_out;
  k_block<false, true><<<256, 512, L_TOTAL, stream>>>(a ST_ARG(4));
}
```

```cpp
#include <hip/hip_runtime.h>
#include <hip/hip_bf16.h>
#include <stdint.h>
#include <type_traits>

#define DI __device__ __forceinline__
#define LAS __attribute__((address_space(3)))
#define GAS __attribute__((address_space(1)))
typedef __bf16 bf16x8 __attribute__((ext_vector_type(8)));
typedef __bf16 bf16x2 __attribute__((ext_vector_type(2)));
typedef short s16x4 __attribute__((ext_vector_type(4)));
typedef float f32x4 __attribute__((ext_vector_type(4)));
typedef float f32x2 __attribute__((ext_vector_type(2)));
typedef unsigned u32x4 __attribute__((ext_vector_type(4)));
typedef unsigned u32x2 __attribute__((ext_vector_type(2)));
typedef unsigned short u16;
typedef __amdgpu_buffer_rsrc_t rsrc_t;
template <int V> using IC = std::integral_constant<int, V>;

constexpr int S_ = 2048, D_ = 1024, F_ = 3584;

DI unsigned pk2(f32x2 v) { return __builtin_bit_cast(unsigned, __builtin_convertvector(v, bf16x2)); }
DI unsigned pk(float lo, float hi) { return pk2((f32x2){lo, hi}); }
DI float bflo(unsigned u) { return __uint_as_float(u << 16); }
DI float bfhi(unsigned u) { return __uint_as_float(u & 0xffff0000u); }
DI float rbf(float x) { return bflo(pk(x, x)); }
DI f32x4 mfma(u32x4 a, u32x4 b, f32x4 c) {
  return __builtin_amdgcn_mfma_f32_16x16x32_bf16(__builtin_bit_cast(bf16x8, a), __builtin_bit_cast(bf16x8, b), c, 0, 0, 0);
}
DI f32x2 lo2(f32x4 a) { return __builtin_shufflevector(a, a, 0, 1); }
DI f32x2 hi2(f32x4 a) { return __builtin_shufflevector(a, a, 2, 3); }
DI f32x4 mad2(f32x4 a, f32x4 b, f32x4 c, f32x4 d) {
  const f32x2 l = __builtin_elementwise_fma(lo2(a), lo2(b), lo2(c) * lo2(d)), h = __builtin_elementwise_fma(hi2(a), hi2(b), hi2(c) * hi2(d));
  return __builtin_shufflevector(l, h, 0, 1, 2, 3);
}
DI f32x4 msb2(f32x4 a, f32x4 b, f32x4 c, f32x4 d) {
  const f32x2 l = __builtin_elementwise_fma(lo2(a), lo2(b), -(lo2(c) * lo2(d))), h = __builtin_elementwise_fma(hi2(a), hi2(b), -(hi2(c) * hi2(d)));
  return __builtin_shufflevector(l, h, 0, 1, 2, 3);
}
DI f32x4 nmad2(f32x4 a, f32x4 b, f32x4 c, f32x4 d) {
  const f32x2 l = __builtin_elementwise_fma(-lo2(a), lo2(b), -(lo2(c) * lo2(d))), h = __builtin_elementwise_fma(-hi2(a), hi2(b), -(hi2(c) * hi2(d)));
  return __builtin_shufflevector(l, h, 0, 1, 2, 3);
}
DI u32x4 zip4(f32x4 x, f32x4 y) { return (u32x4){pk(x[0], y[0]), pk(x[1], y[1]), pk(x[2], y[2]), pk(x[3], y[3])}; }
DI u32x4 cat4(f32x4 x, f32x4 y) { return (u32x4){pk2(lo2(x)), pk2(hi2(x)), pk2(lo2(y)), pk2(hi2(y))}; }
DI float cosr(float rev) { return __builtin_amdgcn_cosf(rev); }
DI float sinr(float rev) { return __builtin_amdgcn_sinf(rev); }
#define WAIT_V(n) asm volatile("s_waitcnt vmcnt(%0)" ::"n"(n) : "memory")
#define WAIT_L0() __builtin_amdgcn_s_waitcnt(0xC07F)
#define FENCE() asm volatile("" ::: "memory")
#define BAR() do { FENCE(); __builtin_amdgcn_s_barrier(); FENCE(); } while (0)
#define LBAR() do { WAIT_L0(); BAR(); } while (0)
#define SCHED() __builtin_amdgcn_sched_barrier(0)
template <int I, int N, class F> DI void sfor(F&& f) { if constexpr (I < N) { f(IC<I>{}); sfor<I + 1, N>(f); } }
template <class T> DI void pin(T& x) { asm volatile("" : "+v"(x)); }
DI u32x2 tr_read(const LAS char* p) { return __builtin_bit_cast(u32x2, __builtin_amdgcn_ds_read_tr16_b64_v4i16((LAS s16x4*)p)); }
DI void set_m0(unsigned base, int imm) { asm volatile("s_add_u32 m0, %0, %1\n\ts_nop 0" ::"s"(base), "i"(imm) : "scc"); }
template <int OFF, bool SC1> DI void glds(unsigned voff, const void* sbase) {
  if (SC1) asm volatile("global_load_lds_dwordx4 %0, %1 offset:%2 sc1" ::"v"(voff), "s"(sbase), "i"(OFF) : "memory");
  else asm volatile("global_load_lds_dwordx4 %0, %1 offset:%2" ::"v"(voff), "s"(sbase), "i"(OFF) : "memory");
}
DI void touch_line(unsigned voff, const void* sbase) {
  asm volatile("global_load_lds_dword %0, %1" ::"v"(voff), "s"(sbase) : "memory");
}
template <int OFF> DI u32x4 ld16_raw(unsigned voff, const void* sbase) {
  u32x4 r; asm volatile("global_load_dwordx4 %0, %1, %2 offset:%3" : "=v"(r) : "v"(voff), "s"(sbase), "i"(OFF) : "memory"); return r;
}
template <int OFF> DI u32x2 ld8_raw(unsigned voff, const void* sbase) {
  u32x2 r; asm volatile("global_load_dwordx2 %0, %1, %2 offset:%3" : "=v"(r) : "v"(voff), "s"(sbase), "i"(OFF) : "memory"); return r;
}
template <int OFF> DI unsigned ld4_raw(unsigned voff, const void* sbase) {
  unsigned r; asm volatile("global_load_dword %0, %1, %2 offset:%3" : "=v"(r) : "v"(voff), "s"(sbase), "i"(OFF) : "memory"); return r;
}
template <int N, class T> DI void wait_v2(T& a, T& b) { asm volatile("s_waitcnt vmcnt(%2)" : "+v"(a), "+v"(b) : "n"(N) : "memory"); }
template <int N0, int N1, class T> DI void wait_v2s(int sel, T& a, T& b) {
  asm volatile("s_cmp_lg_u32 %2, 0\n\ts_cbranch_scc1 1f\n\ts_waitcnt vmcnt(%3)\n1:\n\ts_waitcnt vmcnt(%4)" : "+v"(a), "+v"(b) : "s"(sel), "n"(N0), "n"(N1) : "memory", "scc");
}
template <int SLP = 1> DI void lds_spin(LAS unsigned* w, unsigned want) {
#pragma unroll 1
  for (int sp = 0; sp < (1 << 24); ++sp) {
    if ((unsigned)__builtin_amdgcn_readfirstlane(*(volatile LAS unsigned*)w) >= want) break;
    __builtin_amdgcn_s_sleep(SLP);
  }
  FENCE();
}
DI void flag4_set(LAS unsigned* w) { FENCE(); *(volatile LAS unsigned*)w = 1u; FENCE(); }
template <int SLP = 1> DI void flag4_wait(LAS unsigned* grp) {
#pragma unroll 1
  for (int sp = 0; sp < (1 << 24); ++sp) {
    const u32x4 v = *(volatile LAS u32x4*)grp;
    if (__builtin_amdgcn_readfirstlane(v.x & v.y & v.z & v.w)) break;
    __builtin_amdgcn_s_sleep(SLP);
  }
  FENCE();
}
DI void lds_arrive(LAS unsigned* w) { if ((threadIdx.x & 63) == 0) __hip_atomic_fetch_add(w, 1u, __ATOMIC_RELAXED, __HIP_MEMORY_SCOPE_WORKGROUP); }
DI void st16_out(void* p, u32x4 v) { asm volatile("global_store_dwordx4 %0, %1, off sc1\n\ts_nop 1" ::"v"(p), "v"(v) : "memory"); }
#define CODE_ADDR(sym) ({ unsigned lo_, hi_; \
  asm("s_getpc_b64 s[40:41]\n\ts_add_u32 %0, s40, " #sym "@rel32@lo+4\n\ts_addc_u32 %1, s41, " #sym "@rel32@hi+12" : "=s"(lo_), "=s"(hi_) :: "scc", "s40", "s41"); \
  (const char*)(((unsigned long long)hi_ << 32) | lo_); })
constexpr int BLOCK_CODE_LINES = 224;
DI rsrc_t mkrs(const void* p) { return __builtin_amdgcn_make_buffer_rsrc((void*)p, 0, 0x7fffffff, 0x00020000); }
DI void st16_dev(rsrc_t r, unsigned off, u32x4 v) { __builtin_amdgcn_raw_buffer_store_b128(v, r, off, 0, 16); }
DI void st8_dev(rsrc_t r, unsigned off, u32x2 v) { __builtin_amdgcn_raw_buffer_store_b64(v, r, off, 0, 16); }
DI u32x4 ld16_dev(rsrc_t r, unsigned off) { return __builtin_amdgcn_raw_buffer_load_b128(r, off, 0, 16); }
DI void signal(unsigned* c, unsigned bit) { if ((threadIdx.x & 63) == 0) __hip_atomic_fetch_or(c, bit, __ATOMIC_RELAXED, __HIP_MEMORY_SCOPE_AGENT); }
DI unsigned poll_once(const unsigned* c) {
  unsigned v;
  asm volatile("global_load_dword %0, %1, %2 sc1\n\ts_waitcnt vmcnt(0)" : "=v"(v) : "v"(0u), "s"(c) : "memory");
  return __builtin_amdgcn_readfirstlane(v);
}
template <int SLP = 1> DI void wait_count(const unsigned* c, unsigned want) {
#pragma unroll 1
  for (int sp = 0; sp < (1 << 20); ++sp) {
    if ((poll_once(c) & want) == want) break;
    if (SLP) __builtin_amdgcn_s_sleep(SLP);
  }
}
template <int KEEP = 0> DI bool drained_last(LAS unsigned* cnt, unsigned n) {
  WAIT_V(KEEP);
  unsigned old = 0;
  if ((threadIdx.x & 63) == 0) old = __hip_atomic_fetch_add(cnt, 1u, __ATOMIC_RELAXED, __HIP_MEMORY_SCOPE_WORKGROUP);
  return (unsigned)__builtin_amdgcn_readfirstlane(old) == n - 1;
}

#define ST_PARAM
#define ST_ARG(k)
#define ST_DECL
#define ST_AT(i)
#define ST_FLUSH()
#define ST_FLUSH2()

template <int CTRL> DI float dpp_add(float v) {
  return v + __uint_as_float(__builtin_amdgcn_update_dpp(0u, __float_as_uint(v), CTRL, 0xf, 0xf, true));
}
DI void ln_apply(f32x2 (&v)[8]) {
  const float x0 = __uint_as_float(__builtin_amdgcn_readfirstlane(__float_as_uint(v[0].x)));
#pragma unroll
  for (int i = 0; i < 8; ++i) v[i] = v[i] - x0;
  const f32x2 s2 = ((v[0] + v[1]) + (v[2] + v[3])) + ((v[4] + v[5]) + (v[6] + v[7]));
  const f32x2 qa = v[0] * v[0] + v[1] * v[1], qb = v[2] * v[2] + v[3] * v[3], qc = v[4] * v[4] + v[5] * v[5], qd = v[6] * v[6] + v[7] * v[7];
  const f32x2 q2 = (qa + qb) + (qc + qd);
  const auto sw = __builtin_amdgcn_permlane16_swap(__float_as_uint(s2.x + s2.y), __float_as_uint(q2.x + q2.y), false, false);
  float c = __uint_as_float(sw[0]) + __uint_as_float(sw[1]);
  c = dpp_add<0xB1>(c); c = dpp_add<0x4E>(c); c = dpp_add<0x141>(c); c = dpp_add<0x140>(c);
  const float s = __uint_as_float(__builtin_amdgcn_readlane(__float_as_uint(c), 0)) + __uint_as_float(__builtin_amdgcn_readlane(__float_as_uint(c), 32));
  const float q = __uint_as_float(__builtin_amdgcn_readlane(__float_as_uint(c), 16)) + __uint_as_float(__builtin_amdgcn_readlane(__float_as_uint(c), 48));
  const float md = s * (1.0f / 1024.0f), var = fmaxf(q * (1.0f / 1024.0f) - md * md, 0.f);
  const float rstd = __builtin_amdgcn_rcpf(rbf(__builtin_amdgcn_sqrtf(rbf(rbf(var) + 1e-5f))));
  const float off = (x0 - rbf(x0 + md)) * rstd;
#pragma unroll
  for (int i = 0; i < 8; ++i) v[i] = v[i] * rstd + off;
}

struct RowC { u32x4 Fre, Fim, C0, S0, nS0, C1, nS1, nC1; f32x4 ta[2], tb[2], tc[2], td[2]; };
DI void rowfft_consts(RowC& k, int li, int g) {
  const bool sp = li == 0;
  float c[8], s[8];
#pragma unroll
  for (int j = 0; j < 8; ++j) {
    const int n1 = 4 * j + g; const float a = (float)((n1 * li) & 31) * (1.0f / 32.0f);
    c[j] = cosr(a); s[j] = sp ? ((n1 & 1) ? -1.0f : 1.0f) : -sinr(a);
  }
#pragma unroll
  for (int j = 0; j < 4; ++j) { k.Fre[j] = pk(c[2 * j], c[2 * j + 1]); k.Fim[j] = pk(s[2 * j], s[2 * j + 1]); }
#pragma unroll
  for (int j = 0; j < 8; ++j) { const float a = (float)(((8 * g + j) * li) & 31) * (1.0f / 32.0f); c[j] = cosr(a); s[j] = sinr(a); }
#pragma unroll
  for (int j = 0; j < 4; ++j) { k.C0[j] = pk(c[2 * j], c[2 * j + 1]); k.S0[j] = pk(s[2 * j], s[2 * j + 1]); k.nS0[j] = k.S0[j] ^ 0x80008000u; }
#pragma unroll
  for (int j = 0; j < 8; ++j) { const float a = (float)(((8 * g + j) * (li + 1)) & 31) * (1.0f / 32.0f); c[j] = cosr(a); s[j] = -sinr(a); }
#pragma unroll
  for (int j = 0; j < 4; ++j) { k.C1[j] = pk(c[2 * j], c[2 * j + 1]); k.nS1[j] = pk(s[2 * j], s[2 * j + 1]); k.nC1[j] = k.C1[j] ^ 0x80008000u; }
  const int k1e = sp ? 16 : li;
#pragma unroll
  for (int j = 0; j < 8; ++j) {
    const float a = (float)((8 * g + j) * k1e) * (1.0f / 1024.0f), cc = cosr(a), ss = sinr(a);
    k.ta[j & 1][j >> 1] = sp ? 0.f : cc; k.tb[j & 1][j >> 1] = sp ? cc : ss; k.tc[j & 1][j >> 1] = sp ? 0.f : -ss; k.td[j & 1][j >> 1] = sp ? -ss : cc;
  }
}
DI void pin_consts(RowC& k) {
  pin(k.Fre); pin(k.Fim); pin(k.C0); pin(k.S0); pin(k.nS0); pin(k.C1); pin(k.nS1); pin(k.nC1);
#pragma unroll
  for (int j = 0; j < 2; ++j) { pin(k.ta[j]); pin(k.tb[j]); pin(k.tc[j]); pin(k.td[j]); }
}
template <class PRE> DI void rowfft_run(f32x2 (&v)[8], const RowC& k, LAS unsigned* L, unsigned* z, PRE&& pre_barrier) {
  const int tid = threadIdx.x, wid = tid >> 6, lane = tid & 63;
  const bool sp = (lane & 15) == 0;
  ln_apply(v);
  u32x4 A0, A1;
#pragma unroll
  for (int j = 0; j < 4; ++j) { A0[j] = pk(v[2 * j].x, v[2 * j + 1].x); A1[j] = pk(v[2 * j].y, v[2 * j + 1].y); }
  const f32x4 zero = {0.f, 0.f, 0.f, 0.f};
  const f32x4 r0 = mfma(A0, k.Fre, zero), i0 = mfma(A0, k.Fim, zero), r1 = mfma(A1, k.Fre, zero), i1 = mfma(A1, k.Fim, zero);
  const u32x4 Bre1 = zip4(mad2(k.ta[0], r0, k.tb[0], i0), mad2(k.ta[1], r1, k.tb[1], i1));
  const u32x4 Bim1 = zip4(mad2(k.tc[0], r0, k.td[0], i0), mad2(k.tc[1], r1, k.td[1], i1));
  const u32x4 Braw = zip4(r0, r1);
  u32x4 Bre0, Bim0;
#pragma unroll
  for (int r = 0; r < 4; ++r) { Bre0[r] = sp ? Braw[r] : Bre1[r]; Bim0[r] = sp ? 0u : Bim1[r]; }
  f32x4 xr0 = mfma(k.C0, Bre0, zero), xi0 = mfma(k.nS0, Bre0, zero), xr1 = mfma(k.C1, Bre1, zero), xi1 = mfma(k.nS1, Bre1, zero);
  const f32x4 ny = mfma(k.C1, Bre0, zero);
  xr0 = mfma(k.S0, Bim0, xr0); xi0 = mfma(k.C0, Bim0, xi0); xr1 = mfma(k.nS1, Bim1, xr1); xi1 = mfma(k.nC1, Bim1, xi1);
  const float nyq = __uint_as_float(__builtin_amdgcn_readlane(__float_as_uint(ny[3]), 48));
  xi0[0] = lane == 0 ? nyq : xi0[0];
  const u32x4 o0 = zip4(xr0, xi0), o1 = zip4(xr1, xi1);
  *(LAS u32x4*)(L + wid * 516 + lane * 4) = o0;
  *(LAS u32x4*)(L + wid * 516 + 256 + lane * 4) = o1;
  pre_barrier();
  LBAR();
#pragma unroll
  for (int i = 0; i < 2; ++i) {
    const int ch = tid + 512 * i, sg = ch >> 1, jh = ch & 1;
    u32x4 o;
#pragma unroll
    for (int e = 0; e < 4; ++e) o[e] = L[(4 * jh + e) * 516 + sg];
    st16_out(z + ch * 4, o);
  }
}

constexpr int T3_FRAGS = 15;
constexpr int T2_FRAGS = 20;
DI unsigned table_entry(int idx) {
  const int fr = idx >> 8, lane = (idx >> 2) & 63, e = idx & 3, li = lane & 15, g = lane >> 4;
  if (fr < T3_FRAGS) {
    const int u = fr & 1, n2 = 16 * u + li, r = 4 * g + e;
    if (fr < 10) {
      const int rr = (fr >= 4 && fr < 8) ? r + 1 : r;
      const float a = (float)((n2 * rr) & 31) * (1.0f / 32.0f), c = cosr(a), s = sinr(a);
      if (fr < 2) return pk(c, -s);
      if (fr < 4) return pk(s, c);
      if (fr < 6) return pk(c, -s);
      if (fr < 8) return pk(-s, -c);
      return r == 0 ? pk(1.0f, (li & 1) ? -1.0f : 1.0f) : pk(2.0f * c, -2.0f * s);
    }
    if (fr < 14) { const float a = (float)(n2 * r) * (1.0f / 1024.0f); return __float_as_uint(fr < 12 ? cosr(a) : sinr(a)); }
    const float a = (float)(16 * (e & 1) + li) * (1.0f / 64.0f);
    return __float_as_uint(2.0f * (e < 2 ? cosr(a) : sinr(a)));
  }
  const int f2 = fr - T3_FRAGS;
  if (f2 < 16) {
    const float a = (float)(((16 * (f2 & 3) + 4 * g + e) * (16 * (f2 >> 2) + li)) & 63) * (1.0f / 64.0f);
    return pk(cosr(a), sinr(a));
  }
  const int t = f2 & 1, s0 = 16 * (e >> 1) + 4 * g + 2 * (e & 1);
  const float a0 = (float)((s0 * (16 * t + li)) & 31) * (1.0f / 32.0f), a1 = (float)(((s0 + 1) * (16 * t + li)) & 31) * (1.0f / 32.0f);
  return f2 < 18 ? pk(cosr(a0), cosr(a1)) : pk(sinr(a0), sinr(a1));
}

struct FirstArgs { const u16* x; const u16* wr; const u16* wi; unsigned* z; unsigned* wt; unsigned* wq; unsigned* tab; unsigned* cnt; const u16* w1; };
constexpr int CNT_WORDS = 2 * 128 * 32;

__global__ __launch_bounds__(512) void k_first(FirstArgs p ST_PARAM) {
  __shared__ __attribute__((aligned(16))) char smem_[16640 + 16384 + 256];
  LAS char* smem = (LAS char*)smem_;
  ST_DECL; ST_AT(0);
  const int tid = threadIdx.x, wid = __builtin_amdgcn_readfirstlane(tid >> 6), lane = tid & 63, b = blockIdx.x;
  set_m0((unsigned)(size_t)smem, 16640 + 16384);
  if (b >= 256) {
    const int idx = (b - 256) * 512 + tid;
    if (idx < (T3_FRAGS + T2_FRAGS) * 256) p.tab[idx] = table_entry(idx);
    if (idx < CNT_WORDS) p.cnt[idx] = 0u;
    if (b == 256) {
      if (tid < BLOCK_CODE_LINES) { touch_line(tid * 128, CODE_ADDR(k_block0)); touch_line(tid * 128, CODE_ADDR(k_block1)); }
      if (tid < 24) touch_line(tid * 128, CODE_ADDR(k_col));
    }
    WAIT_V(0);
    return;
  }
  unsigned u[8];
  {
    const u16* xr = p.x + (size_t)(256 * (b >> 5) + 32 * wid + (b & 31)) * D_;
    sfor<0, 8>([&](auto J) { u[J] = ld4_raw<J * 256>(lane * 4, xr); });
  }
  const int kb = (b >> 3) & 7, fw = b >> 6, tg = b & 7;
  const int fi = wid * 8 + (lane >> 3), kc = lane & 7;
  const unsigned wo = ((16 * fw + (fi >> 2) + 64 * (4 * tg + (fi & 3))) * 513 + 64 * kb + 8 * kc) * 2;
  u32x4 wr = ld16_raw<0>(wo & ~3u, p.wr), wi = ld16_raw<0>(wo & ~3u, p.wi);
  unsigned wr4 = ld4_raw<16>(wo & ~3u, p.wr), wi4 = ld4_raw<16>(wo & ~3u, p.wi);
  const int sidx = b * 8 + (lane >> 3);
  unsigned se;
  {
    const int ln = (sidx >> 2) & 63, f = 16 * (sidx >> 9) + (ln & 15) + 64 * (16 * ((sidx >> 8) & 1) + 4 * (ln >> 4) + (sidx & 3));
    const u16* src = ((kc & 2) ? p.wi : p.wr) + ((kc & 1) ? (2048 - f) & 2047 : f) * 513 + (kc & 4) * 128;
    asm volatile("global_load_ushort %0, %1, off" : "=v"(se) : "v"(wid == 7 ? src : p.wr) : "memory");
  }
  RowC k;
  rowfft_consts(k, lane & 15, lane >> 4);
  pin_consts(k);
  ST_AT(1);
  asm volatile("s_waitcnt vmcnt(5)" : "+v"(u[0]), "+v"(u[1]), "+v"(u[2]), "+v"(u[3]), "+v"(u[4]), "+v"(u[5]), "+v"(u[6]), "+v"(u[7]) :: "memory");
  ST_AT(2);
  if (lane < 28) touch_line(((((b >> 2) & 1) * 512 + (b >> 3) * 16 + wid * 2 + (lane >= 14)) * F_ + (b & 3) * 896) * 2 + (lane >= 14 ? lane - 14 : lane) * 128, p.w1);
  f32x2 v[8];
#pragma unroll
  for (int j = 0; j < 8; ++j) v[j] = (f32x2){bflo(u[j]), bfhi(u[j])};
  LAS unsigned* W = (LAS unsigned*)(smem + 16640);
  rowfft_run(v, k, (LAS unsigned*)smem, p.z + (size_t)b * 4096, [&]() {
    wait_v2<1>(wr, wi);
    pin(se); pin(wr4); pin(wi4);
    const unsigned sh = (wo & 2) * 8;
#pragma unroll
    for (int e = 0; e < 4; ++e) {
      wr[e] = __builtin_amdgcn_alignbit(e < 3 ? wr[e < 3 ? e + 1 : 0] : wr4, wr[e], sh);
      wi[e] = __builtin_amdgcn_alignbit(e < 3 ? wi[e < 3 ? e + 1 : 0] : wi4, wi[e], sh);
    }
#pragma unroll
    for (int e = 0; e < 8; ++e)
      W[(8 * kc + e) * 64 + (((fi >> 2) ^ kc) << 2) + (fi & 3)] = __builtin_amdgcn_perm(wi[e >> 1], wr[e >> 1], (e & 1) ? 0x07060302u : 0x05040100u);
    if (wid == 7) {
      const float e = bflo(se);
      float h = (kc & 3) == 3 ? -e : e;
      h = dpp_add<0xB1>(h);
      const float o = __uint_as_float(__builtin_amdgcn_update_dpp(0u, __float_as_uint(h), 0x141, 0xf, 0xf, true));
      const float im0 = __uint_as_float(__builtin_amdgcn_update_dpp(0u, __float_as_uint(h), 0x4E, 0xf, 0xf, true));
      const float re1 = __uint_as_float(__builtin_amdgcn_update_dpp(0u, __float_as_uint(o), 0x4E, 0xf, 0xf, true));
      if (kc == 0) { p.wt[sidx] = pk((h + re1) * 0.25f, (im0 + o) * 0.25f); p.wq[sidx] = pk((h - re1) * 0.25f, (im0 - o) * 0.25f); }
    }
  });
#pragma unroll
  for (int i = 0; i < 2; ++i) {
    const int idx = tid + 512 * i, kl = idx >> 4, f1l = idx & 15, kk = 64 * kb + kl;
    const int k1 = kk & 31, k2 = kk >> 5;
    const int t = k1 >= 16, l1 = (k1 <= 16) ? (k1 & 15) : 32 - k1;
    const int sigma = t * 256 + (l1 + 16 * (k2 >> 2)) * 4 + (k2 & 3);
    const u32x4 o = *(const LAS u32x4*)(W + kl * 64 + ((f1l ^ (kl >> 3)) << 2));
    if (kk != 0) st16_out(p.wt + ((sigma * 4 + fw) * 2 + (tg >> 2)) * 256 + ((tg & 3) * 16 + f1l) * 4, o);
  }
  ST_AT(3);
  WAIT_V(0);
  ST_AT(4); ST_FLUSH();
}
constexpr int FIRST_BLOCKS = 256 + ((T3_FRAGS + T2_FRAGS) * 256 + 511) / 512;
static_assert(CNT_WORDS <= (FIRST_BLOCKS - 256) * 512, "counter reset");

extern "C" __global__ __launch_bounds__(512) void k_col(const unsigned* __restrict__ zin, unsigned* __restrict__ zout, const unsigned* __restrict__ wt,
                                             const unsigned* __restrict__ wq, const unsigned* __restrict__ tab ST_PARAM) {
  __shared__ __attribute__((aligned(16))) char smem_[32768 + T2_FRAGS * 1024 + 32768];
  LAS char* smem = (LAS char*)smem_;
  ST_DECL; ST_AT(0);
  const int tid = threadIdx.x, wid = __builtin_amdgcn_readfirstlane(tid >> 6), lane = tid & 63, li = lane & 15, g = lane >> 4;
  const int pb = (blockIdx.x & 7) * 32 + (blockIdx.x >> 3), c = wid >> 2, w = wid & 3, sigma = 2 * pb + c;
  {
    const unsigned lb = (unsigned)(size_t)smem;
    const int P = wid * 128 + lane, beta = P >> 2, ch = (P & 3) ^ ((0x78 >> (2 * ((beta >> 2) & 3))) & 3);
    const unsigned vo = beta * 16384 + ch * 16;
    set_m0(lb + wid * 2048, 32768);
    glds<0, false>(lane * 16, tab + T3_FRAGS * 256 + wid * 512);
    glds<1024, false>(lane * 16, tab + T3_FRAGS * 256 + wid * 512);
    if (wid < 4) { set_m0(lb + wid * 1024, 32768 + 16384); glds<0, false>(lane * 16, tab + (T3_FRAGS + 16) * 256 + wid * 256); }
    set_m0(lb + wid * 2048, 0);
    glds<0, false>(vo, zin + pb * 16);
    glds<1024, false>(vo + 16 * 16384 - 1024, zin + pb * 16);
  }
  unsigned* zo = zout + (size_t)(32 * (2 * w + (g >> 1)) + li) * 4096 + pb * 16 + (2 * c + (g & 1)) * 4;
  pin(zo);
  u32x4 Wv[2], Qv[2];
  Wv[0] = ld16_raw<0>(lane * 16, wt + (sigma * 4 + w) * 512);
  Wv[1] = ld16_raw<1024>(lane * 16, wt + (sigma * 4 + w) * 512);
  LAS f32x4* tw = (LAS f32x4*)(smem + 32768 + T2_FRAGS * 1024) + w * 512 + lane;
  {
    const int m0 = c ? 16 * w + 4 * g : 4 * g, m1 = c ? 0 : 16, n0 = c ? li : 16 * w + li, n1 = c ? 16 : 0;
#pragma unroll
    for (int h = 0; h < 2; ++h) {
      f32x4 cc, ss;
#pragma unroll
      for (int r = 0; r < 4; ++r) { const float a = (float)((m0 + h * m1 + r) * (n0 + h * n1)) * (1.0f / 2048.0f); cc[r] = cosr(a); ss[r] = sinr(a); }
      const float sc = c ? 4.76837158203125e-07f : 1.0f;
      tw[(c * 4 + h * 2) * 64] = cc * sc; tw[(c * 4 + h * 2 + 1) * 64] = ss * sc;
    }
  }
  WAIT_L0();
  WAIT_V(4);
  BAR();
  f32x4 t1c[2], t1s[2], t2c[2], t2s[2];
#pragma unroll
  for (int h = 0; h < 2; ++h) { t1c[h] = tw[h * 128]; t1s[h] = tw[h * 128 + 64]; t2c[h] = tw[256 + h * 128]; t2s[h] = tw[256 + h * 128 + 64]; pin(t1c[h]); pin(t1s[h]); pin(t2c[h]); pin(t2s[h]); }
  u32x4 Ere[4], Eim[4], Gc[2], Gs[2], Gn[2], Z[4][2];
#pragma unroll
  for (int k = 0; k < 4; ++k) Ere[k] = *(const LAS u32x4*)(smem + 32768 + (w * 4 + k) * 1024 + lane * 16);
#pragma unroll
  for (int t = 0; t < 2; ++t) {
    Gc[t] = *(const LAS u32x4*)(smem + 32768 + (16 + t) * 1024 + lane * 16);
    Gs[t] = *(const LAS u32x4*)(smem + 32768 + (18 + t) * 1024 + lane * 16);
  }
#pragma unroll
  for (int k = 0; k < 4; ++k) {
#pragma unroll
    for (int e = 0; e < 4; ++e) Eim[k][e] = __builtin_amdgcn_alignbit(Ere[k][e], Ere[k][e], 16) ^ 0x8000u;
    pin(Eim[k]);
  }
  Gn[0] = Gs[0] ^ 0x80008000u; Gn[1] = Gs[1] ^ 0x80008000u;
  pin(Gn[0]); pin(Gn[1]); pin(Gc[0]); pin(Gc[1]);
  const LAS char* zb = smem + (32 * (g >> 1) + li) * 64 + (((2 * c + (g & 1)) ^ ((0x78 >> (2 * (li >> 2))) & 3)) << 4);
  pin(zb);
  ST_AT(1);
  WAIT_V(2);
  BAR();
  ST_AT(2);
#pragma unroll
  for (int k = 0; k < 4; ++k) { Z[k][0] = *(const LAS u32x4*)(zb + k * 4096); Z[k][1] = *(const LAS u32x4*)(zb + k * 4096 + 1024); }
  SCHED();
  const f32x4 zero = {0.f, 0.f, 0.f, 0.f};
  f32x4 dr[2] = {zero, zero}, di[2] = {zero, zero};
#pragma unroll
  for (int k = 0; k < 4; ++k) {
    dr[0] = mfma(Z[k][0], Ere[k], dr[0]); di[0] = mfma(Z[k][0], Eim[k], di[0]);
    dr[1] = mfma(Z[k][1], Ere[k], dr[1]); di[1] = mfma(Z[k][1], Eim[k], di[1]);
  }
  const u32x4 Bre = cat4(mad2(dr[0], t1c[0], di[0], t1s[0]), mad2(dr[1], t1c[1], di[1], t1s[1]));
  const u32x4 Bim = cat4(msb2(di[0], t1c[0], dr[0], t1s[0]), msb2(di[1], t1c[1], dr[1], t1s[1]));
  f32x4 xr[2], xi[2];
#pragma unroll
  for (int t = 0; t < 2; ++t) { xr[t] = mfma(Gc[t], Bre, zero); xi[t] = mfma(Gn[t], Bre, zero); }
#pragma unroll
  for (int t = 0; t < 2; ++t) { xr[t] = mfma(Gs[t], Bim, xr[t]); xi[t] = mfma(Gc[t], Bim, xi[t]); }
  f32x4 ur[2], ui[2];
  SCHED();
  wait_v2<0>(Wv[0], Wv[1]);
#pragma unroll
  for (int t = 0; t < 2; ++t) {
    const f32x4 a = __builtin_bit_cast(f32x4, Wv[t] << 16), b = __builtin_bit_cast(f32x4, Wv[t] & 0xffff0000u);
    ur[t] = msb2(xr[t], a, xi[t], b); ui[t] = mad2(xr[t], b, xi[t], a);
  }
  if (pb == 0) {
    Qv[0] = *(const u32x4*)(wq + (w * 2 + 0) * 256 + lane * 4);
    Qv[1] = *(const u32x4*)(wq + (w * 2 + 1) * 256 + lane * 4);
    LAS f32x2* X = (LAS f32x2*)smem;
    LBAR();
    if (c == 0) {
#pragma unroll
      for (int t = 0; t < 2; ++t)
#pragma unroll
        for (int r = 0; r < 4; ++r) X[16 * w + li + 64 * (16 * t + 4 * g + r)] = (f32x2){xr[t][r], xi[t][r]};
    }
    LBAR();
    if (c == 0) {
#pragma unroll
      for (int t = 0; t < 2; ++t)
#pragma unroll
        for (int r = 0; r < 4; ++r) {
          const f32x2 m = X[(2048 - (16 * w + li + 64 * (16 * t + 4 * g + r))) & 2047];
          const float a = bflo(Qv[t][r]), b = bfhi(Qv[t][r]);
          ur[t][r] += m.x * a + m.y * b; ui[t][r] += m.x * b - m.y * a;
        }
    }
  }
  const u32x4 Are = cat4(ur[0], ur[1]), Aim = cat4(ui[0], ui[1]);
  LAS char* ex = smem + 16384 + c * 8192;
  {
    f32x4 tr[2], ti[2];
#pragma unroll
    for (int t = 0; t < 2; ++t) { tr[t] = mfma(Are, Gc[t], zero); ti[t] = mfma(Are, Gs[t], zero); }
#pragma unroll
    for (int t = 0; t < 2; ++t) { tr[t] = mfma(Aim, Gn[t], tr[t]); ti[t] = mfma(Aim, Gc[t], ti[t]); }
#pragma unroll
    for (int t = 0; t < 2; ++t)
      *(LAS u32x4*)(ex + (w * 2 + t) * 1024 + lane * 16) = zip4(msb2(tr[t], t2c[t], ti[t], t2s[t]), nmad2(tr[t], t2s[t], ti[t], t2c[t]));
  }
  ST_AT(3);
  LBAR();
  ST_AT(4);
  {
#pragma unroll
    for (int k = 0; k < 4; ++k) { Z[k][0] = *(const LAS u32x4*)(ex + (k * 2) * 1024 + lane * 16); Z[k][1] = *(const LAS u32x4*)(ex + (k * 2 + 1) * 1024 + lane * 16); }
    f32x4 orr[2] = {zero, zero}, oi[2] = {zero, zero};
#pragma unroll
    for (int k = 0; k < 4; ++k)
#pragma unroll
      for (int t = 0; t < 2; ++t) { orr[t] = mfma(Ere[k], Z[k][t], orr[t]); oi[t] = mfma(Eim[k], Z[k][t], oi[t]); }
#pragma unroll
    for (int t = 0; t < 2; ++t) *(u32x4*)(zo + t * 65536) = zip4(orr[t], -oi[t]);
  }
  ST_AT(5); ST_FLUSH();
}

template <int BN, int KT, int NS, int A0, int ASTR, int B0, int BSTR, int BROT, bool AROW>
struct Mma {
  static constexpr int MB = 4, NT = BN / 32, NM = MB * NT, PITCH = BN * 2, NR = MB + 2 * NT;
  static constexpr int NBB = BN == 224 ? 2 : NT;
  static constexpr int SPB = (NS * BSTR <= 65536) ? NS : 2, NBS = (NS + SPB - 1) / SPB;
  static DI int swz(int k) { return BN == 224 ? ((k >> 2) & 1) << 1 : (k & 7) << 1; }
  template <int NOBAR, class START, class INIT, class FREED, class HOOK>
  static DI void run(LAS char* smem, f32x4 (&acc)[NT][MB], int wm, int wn, START&& start, INIT&& init, FREED&& freed, HOOK&& hook) {
    const int lane = threadIdx.x & 63, g = lane >> 4;
    const LAS char *pa[2], *pb[NBS][NBB];
    pa[0] = smem + A0 + wm * MB * 2048 + (AROW ? (lane & 15) * 128 + ((g ^ (lane & 7)) << 4) : lane * 16); pin(pa[0]);
    if (AROW) { pa[1] = smem + A0 + wm * MB * 2048 + (lane & 15) * 128 + (((4 + g) ^ (lane & 7)) << 4); pin(pa[1]); }
    {
      const int q = (lane >> 2) & 3, pp = lane & 3, r = 4 * g + q;
#pragma unroll
      for (int s = 0; s < NBS; ++s)
#pragma unroll
        for (int n = 0; n < NBB; ++n) { pb[s][n] = smem + B0 + s * SPB * BSTR + r * PITCH + (((2 * (wn * NT + n) + (pp >> 1)) ^ swz(r)) << 4) + 8 * (pp & 1); pin(pb[s][n]); }
    }
    u32x4 Fa[2][MB]; u32x2 Fb[2][NT][2];
    auto rd1 = [&](auto SET, auto ST, auto KS, auto J) {
      constexpr int set = SET, st = ST, ks = KS, j = J;
      if constexpr (j >= 2 && j < 2 + MB) Fa[set][j - 2] = *(const LAS u32x4*)(pa[AROW ? ks : 0] + st * ASTR + (j - 2) * 2048 + (AROW ? 0 : ks * 1024));
      else {
        constexpr int jj = j < 2 ? j : j - MB, n = jj >> 1, h = jj & 1;
        constexpr int nb = NBB == NT ? n : (n & 1), extra = NBB == NT ? 0 : (n & ~1) * 32;
        constexpr int sb = (st + BROT) % NS;
        Fb[set][n][h] = tr_read(pb[sb / SPB][nb] + (sb % SPB) * BSTR + extra + ks * 32 * PITCH + h * 16 * PITCH);
      }
    };
    auto mm1 = [&](auto SET, auto N, auto M) {
      constexpr int set = SET, n = N, m = M;
      const u32x4 b = {Fb[set][n][0].x, Fb[set][n][0].y, Fb[set][n][1].x, Fb[set][n][1].y};
      acc[n][m] = mfma(b, Fa[set][m], acc[n][m]);
    };
    auto phase = [&](auto SET, auto ST, auto KS) {
      sfor<0, NM>([&](auto I) {
        constexpr int i = I;
        if constexpr (i == NM - 1) { WAIT_L0(); SCHED(); }
        mm1(SET, IC<i / MB>{}, IC<((i / MB) & 1) ? MB - 1 - i % MB : i % MB>{});
        SCHED();
        if constexpr (i < NR) rd1(IC<1 - SET>{}, ST, KS, I);
        SCHED();
      });
    };
    auto tile = [&](auto S, auto NB) {
      using NX = IC<(S + 1) % NS>;
      if constexpr (NB) { FENCE(); freed(S); FENCE(); } else BAR();
      SCHED();
      phase(IC<1>{}, NX{}, IC<0>{});
      phase(IC<0>{}, NX{}, IC<1>{});
    };
    start();
    sfor<0, NR>([&](auto J) { rd1(IC<0>{}, IC<0>{}, IC<0>{}, J); });
    init();
    SCHED();
    phase(IC<0>{}, IC<0>{}, IC<1>{});
    constexpr int T0 = NOBAR > 0 ? 1 : 0;
    if constexpr (NOBAR > 0) sfor<0, NS>([&](auto S) { tile(S, std::bool_constant<(S < NOBAR)>{}); });
    for (int t = T0; t < (KT - 1) / NS; ++t) { hook(t); SCHED(); sfor<0, NS>([&](auto S) { tile(S, std::false_type{}); }); }
    sfor<0, (KT - 1) % NS>([&](auto S) { tile(S, std::false_type{}); });
    BAR();
    SCHED();
    sfor<0, NM>([&](auto I) { mm1(IC<1>{}, IC<I % NT>{}, IC<I / NT>{}); });
    SCHED();
  }
};

constexpr int L_A = 0, L_G1B = 49152, L_G2B = 65536, L_BIAS = 135168, L_CNT = L_BIAS + 512, L_JUNK = L_BIAS + 768, L_X1 = 136192, L_TOTAL = L_X1 + 16384;
constexpr int L_ZT = 0, L_TAB = 16640;
typedef Mma<224, 16, 3, L_A, 16384, L_G1B, 28672, 0, true> M1;
typedef Mma<128, 28, 4, L_A, 16384, L_G2B, 16384, 1, false> M2;

struct BlockArgs {
  const unsigned* zin; const u16* xin; const unsigned* tab;
  const u16* w1; const u16* b1; const u16* w2; const u16* b2;
  u16* xn; u16* h; u16* part; unsigned* cnt;
  u16* xout;
  unsigned* zout; const unsigned* wt;
};

template <bool FIRST, bool LAST>
DI void block_body(const BlockArgs& p, const char* code ST_PARAM) {
  extern __shared__ __attribute__((aligned(16))) char dsm[];
  LAS char* smem = (LAS char*)dsm;
  ST_DECL; ST_AT(0);
  const int tid = threadIdx.x, wid = __builtin_amdgcn_readfirstlane(tid >> 6), lane = tid & 63, li = lane & 15, g = lane >> 4;
  const int bx = blockIdx.x & 7, bi = blockIdx.x >> 3;
  const int mt = (bx >> 2) * 8 + (bi & 7), jt = (bx & 3) * 4 + (bi >> 3);
  const int beta = mt * 16 + jt;
  const unsigned lds0 = (unsigned)(size_t)smem;
  LAS unsigned* lcnt = (LAS unsigned*)(smem + L_CNT);
  unsigned* c_xn = p.cnt + (mt * 8 + 0) * 32; unsigned* c_p = p.cnt + (mt * 8 + 1) * 32;
  const unsigned bit16 = 1u << jt, oth16 = 0xffffu & ~bit16, bit8 = 1u << (jt & 7), oth8 = 0xffu & ~bit8;
  unsigned* c_h0 = p.cnt + (mt * 8 + 2 + (jt >> 3)) * 32; unsigned* c_h1 = c_h0 + 64; unsigned* c_h2 = c_h0 + 128;

  const int lw = wid & 3;
  const char* g1B = (const char*)p.w1 + ((size_t)(lw * 16) * F_ + jt * 224) * 2 - 4096;
  auto g1_off = [&](unsigned (&vb)[7]) {
    int l2 = lane; pin(l2);
    int r = (l2 * 37) >> 10, c = l2 - r * 28;
#pragma unroll
    for (int i = 0; i < 7; ++i) {
      vb[i] = r * (F_ * 2) + ((c ^ M1::swz(r)) << 4) + 4096 - (i & 3) * 1024;
      r += 2; c += 8; if (c >= 28) { c -= 28; r += 1; }
    }
  };
  auto g1_tB = [&](const unsigned (&vb)[7], int s) {
    sfor<0, 7>([&](auto I) { constexpr int i = I;
      if constexpr ((i & 3) == 0) set_m0(lds0 + L_G1B + lw * 7168 + s * 28672, i * 1024);
      glds<(i & 3) * 1024, false>(vb[i], g1B); });
    g1B += (size_t)64 * F_ * 2;
  };

  {
    u32x4 zi[2];
    zi[0] = ld16_raw<0>(tid * 16, p.zin + (size_t)beta * 4096);
    zi[1] = ld16_raw<0>(tid * 16, p.zin + (size_t)beta * 4096 + 2048);
    set_m0(lds0 + wid * 2048, L_TAB);
    glds<0, false>(lane * 16, p.tab + wid * 512);
    if (wid < 7) glds<1024, false>(lane * 16, p.tab + wid * 512);
    if (tid < 48) lcnt[tid] = 0u;
    unsigned vb[7];
    if (wid >= 4) {
      if (lw > 0) { set_m0(lds0, L_JUNK); touch_line((BLOCK_CODE_LINES - 192 + (lw - 1) * 64 + lane) * 128, code); }
      g1_off(vb);
      if (lw == 0) { set_m0(lds0, L_BIAS); if (lane < 28) glds<0, false>(lane * 16, p.b1 + jt * 224); }
      g1_tB(vb, 0);
    }
    wait_v2s<0, 7>(wid >> 2, zi[0], zi[1]);
    u32x4 res[2]; u32x2 rs[4];
    if (FIRST) {
      const u16* xr = p.xin + (size_t)(256 * (beta >> 5) + 32 * wid + (beta & 31)) * D_;
      rs[0] = ld8_raw<0>(li * 64 + g * 8, xr); rs[1] = ld8_raw<32>(li * 64 + g * 8, xr);
      rs[2] = ld8_raw<1024>(li * 64 + g * 8, xr); rs[3] = ld8_raw<1056>(li * 64 + g * 8, xr);
    } else {
      const u16* xr = p.xin + (size_t)(beta * 8 + wid) * D_;
      res[0] = ld16_raw<0>(li * 64 + g * 16, xr); res[1] = ld16_raw<1024>(li * 64 + g * 16, xr);
    }
    LAS unsigned* L = (LAS unsigned*)(smem + L_ZT);
#pragma unroll
    for (int i = 0; i < 2; ++i) {
      const int ch = tid + 512 * i, sg = ch >> 1, jh = ch & 1;
#pragma unroll
      for (int e = 0; e < 4; ++e) L[(4 * jh + e) * 516 + sg] = zi[i][e];
    }
    LBAR();
    ST_AT(1);
    const u32x4 X0 = *(const LAS u32x4*)(L + wid * 516 + lane * 4), X1 = *(const LAS u32x4*)(L + wid * 516 + 256 + lane * 4);
    u32x4 T[T3_FRAGS];
#pragma unroll
    for (int f = 0; f < T3_FRAGS; ++f) T[f] = *(const LAS u32x4*)(smem + L_TAB + f * 1024 + lane * 16);
    SCHED();
    if (wid >= 4) g1_tB(vb, 1);
    const unsigned dn = __builtin_amdgcn_readfirstlane(X0[0]);
    const float dc = bflo(dn), nyq = bfhi(dn);
    const f32x4 zero = {0.f, 0.f, 0.f, 0.f};
    const f32x4 hh = __builtin_bit_cast(f32x4, T[14]);
    f32x4 p0r[2], p0i[2], vr[2], vi[2];
#pragma unroll
    for (int u = 0; u < 2; ++u) { p0r[u] = mfma(X0, T[u], zero); p0i[u] = mfma(X0, T[2 + u], zero); }
#pragma unroll
    for (int u = 0; u < 2; ++u) { vr[u] = mfma(X1, T[4 + u], p0r[u]); vi[u] = mfma(X1, T[6 + u], p0i[u]); }
    const float dn2 = ((li & 1) ? -nyq : nyq) - dc;
    u32x4 A[2];
#pragma unroll
    for (int u = 0; u < 2; ++u) {
      const f32x4 twc = __builtin_bit_cast(f32x4, T[10 + u]), tws = __builtin_bit_cast(f32x4, T[12 + u]);
      f32x4 x = msb2(vr[u], twc, vi[u], tws), y = mad2(vr[u], tws, vi[u], twc);
      const float sx = 2.0f * p0r[u][0] + dn2;
      const float sy = hh[u] * (vr[u][0] - p0r[u][0]) - hh[2 + u] * (vi[u][0] - p0i[u][0]);
      x[0] = g == 0 ? sx : x[0]; y[0] = g == 0 ? sy : y[0];
      A[u] = zip4(x, y);
    }
    if (FIRST) {
      wait_v2s<0, 7>(wid >> 2, rs[0], rs[1]); wait_v2<7>(rs[2], rs[3]);
      res[0] = (u32x4){rs[0].x, rs[0].y, rs[1].x, rs[1].y}; res[1] = (u32x4){rs[2].x, rs[2].y, rs[3].x, rs[3].y};
    } else {
      wait_v2s<0, 7>(wid >> 2, res[0], res[1]);
    }
    f32x2 v[8];
#pragma unroll
    for (int vv = 0; vv < 2; ++vv) {
      const u32x4 cb = T[8 + vv];
#pragma unroll
      for (int u = 0; u < 2; ++u) {
        const f32x4 r4 = {bflo(res[vv][2 * u]), bfhi(res[vv][2 * u]), bflo(res[vv][2 * u + 1]), bfhi(res[vv][2 * u + 1])};
        const f32x4 y = mfma(A[u], cb, r4);
        v[4 * vv + 2 * u] = (f32x2){y[0], y[1]}; v[4 * vv + 2 * u + 1] = (f32x2){y[2], y[3]};
      }
      const int n1 = 16 * vv + li;
      *(LAS u32x4*)(smem + L_X1 + n1 * 512 + (((wid * 4 + g) ^ (n1 & 7)) << 4)) = (u32x4){pk2(v[4 * vv]), pk2(v[4 * vv + 1]), pk2(v[4 * vv + 2]), pk2(v[4 * vv + 3])};
    }
    ln_apply(v);
    const rsrc_t rx = mkrs(p.xn + (size_t)(beta * 8 + wid) * D_);
#pragma unroll
    for (int vv = 0; vv < 2; ++vv)
      st16_dev(rx, vv * 1024 + li * 64 + g * 16, (u32x4){pk2(v[4 * vv]), pk2(v[4 * vv + 1]), pk2(v[4 * vv + 2]), pk2(v[4 * vv + 3])});
    ST_AT(2);
    if (drained_last<0>(lcnt + 0, 8)) signal(c_xn, bit16);
    ST_AT(14);
  }

  RowC rc;
  if (wid >= 4) {
    __builtin_amdgcn_s_setprio(3);
    int ktab;
    { const int e = lane < 21 ? lane - 7 : lane - 21, l = lane - 35, l3 = (l * 43) >> 7, lr = l - 3 * l3;
      const int d = lane < 35 ? e >> 1 : l3;
      const int o = lane < 35 ? (e & 1) * 4 + (lane >= 21) : (lr == 0 ? 2 : lr == 1 ? 6 : 3);
      ktab = lane < 7 ? 7 * (jt & 7) + lane : 7 * (d + (d >= (jt & 7))) + o; }
    unsigned vb0, vb1, vb2, vb3, va = lane * 16;
    { const int r = lane >> 4, c = lane & 15;
      vb0 = r * 2048 + ((c ^ M2::swz(r)) << 4); vb1 = (r + 4) * 2048 + ((c ^ M2::swz(r + 4)) << 4) - 1024; vb2 = vb0 + 16384 - 2048; vb3 = vb1 + 16384 - 2048; }
    const LAS char* hsrc = smem + L_A + (lw & 1) * 8192 + lane * 16;
    pin(ktab); pin(vb0); pin(vb1); pin(vb2); pin(vb3); pin(va); pin(hsrc);
      const char* g2A = (const char*)p.h + ((size_t)(mt * 8) * 112 + (jt >> 3) * 56) * 1024;
      const char* g2B = (const char*)p.w2 + ((size_t)((jt >> 3) * 1792) * D_ + (jt & 7) * 128) * 2;
      auto tA2 = [&](int t, int l) {
        const char* a = g2A + (__builtin_amdgcn_readlane(ktab, 2 * t + (l >> 1)) + (l & 1) * 448) * 1024;
        sfor<0, 4>([&](auto I) { constexpr int i = I; set_m0(lds0 + L_A + (l & 1) * 8192 + (l >> 1) * 1024 + (t & 3) * 16384, i * 2048); glds<0, true>(va, a + i * 112 * 1024); });
      };
      auto tB2 = [&](int t, int l) {
        const char* b = g2B + (size_t)(__builtin_amdgcn_readlane(ktab, 2 * t + (l >> 1)) * 2 + (l & 1)) * 32768;
        set_m0(lds0 + L_G2B + l * 4096 + ((t + 1) & 3) * 16384, 0);
        glds<0, false>(vb0, b); glds<1024, false>(vb1, b); glds<2048, false>(vb2, b); glds<3072, false>(vb3, b);
      };
    {
      const char* gA = (const char*)p.xn + (size_t)(mt * 128 + lw * 32) * 2048;
      const unsigned lA = lds0 + L_A + lw * 4096;
      const unsigned va0 = (lane >> 3) * 2048 + (((lane & 7) ^ (lane >> 3)) << 4), va1 = va0 + 15360, va2 = va0 + 2 * 15360, va3 = va0 + 3 * 15360;
      auto tA = [&](int s) {
        set_m0(lA + s * 16384, 0);
        glds<0, true>(va0, gA); glds<1024, true>(va1, gA); glds<2048, true>(va2, gA); glds<3072, true>(va3, gA);
        gA += 128;
      };
      unsigned vb[7];
      g1_off(vb);
      g1_tB(vb, 2);
      ST_AT(13);
      LBAR();
      tA(0); tA(1); tA(2);
      WAIT_V(8); BAR();
      WAIT_V(4); BAR(); tA(0); g1_tB(vb, 0);
      WAIT_V(11); BAR(); tA(1); g1_tB(vb, 1);
      int cur = 2;
      for (int t = 2; t < 13; ++t) {
        WAIT_V(11); BAR(); tA(cur); g1_tB(vb, cur); cur = cur == 2 ? 0 : cur + 1;
        if (FIRST && t == 3 + 2 * lw) {
          set_m0(lds0, L_JUNK);
          if (lane < 56) touch_line((lw * 7 + (lane >> 3)) * 2048 + (lane & 7) * 128,
                                    (const char*)p.w2 + ((size_t)((jt >> 3) * 1792 + (bx >> 2) * 896 + bi * 28) * D_ + (bx & 1) * 512) * 2);
        }
      }
      WAIT_V(11); BAR(); tB2(0, lw);
      WAIT_V(4); BAR(); tB2(1, lw); tB2(2, lw);
      WAIT_V(0);
      BAR();
      ST_AT(3);
    }
    {
      ST_AT(4);
      const rsrc_t rh = mkrs(p.h + ((size_t)(mt * 8 + (lw & 1) * 4) * 112 + jt * 7) * 512);
      tB2(3, lw);
      {
        flag4_wait(lcnt + 16);
        u32x4 o[4];
#pragma unroll
        for (int i = 0; i < 4; ++i) o[i] = *(const LAS u32x4*)(hsrc + (lw >> 1) * 32768 + i * 2048);
#pragma unroll
        for (int i = 0; i < 4; ++i) __builtin_amdgcn_raw_buffer_store_b128(o[i], rh, va, (i * 112 + (lw >> 1) * 4) * 1024, 16);
        ST_AT(5);
        if (drained_last<0>(lcnt + 4, 4)) signal(c_h0, bit8);
        ST_AT(6);
      }
      if (lw >= 2) {
        sfor<1, 4>([&](auto Q) { constexpr int q = Q, n = q == 3 ? 4 : 8;
          flag4_wait<2>(lcnt + 16 + 4 * q);
          u32x4 o[n];
#pragma unroll
          for (int i = 0; i < n; ++i) { const int kb = q + (i & 4); o[i] = *(const LAS u32x4*)(hsrc + (kb >> 1) * 16384 + (i & 3) * 2048 + (kb & 1) * 1024); }
          if (q == 1) { WAIT_L0(); FENCE(); lds_arrive(lcnt + 12); }
#pragma unroll
          for (int i = 0; i < n; ++i) __builtin_amdgcn_raw_buffer_store_b128(o[i], rh, va, ((i & 3) * 112 + q + (i & 4)) * 1024, 16);
          ST_AT(6 + q);
          if (q == 2 && drained_last<8>(lcnt + 5, 2)) signal(c_h1, bit8);
        });
      } else {
        if (lw == 0) { wait_count<0>(c_h0, oth8); if (lane == 0) lcnt[3] = 1u; } else lds_spin(lcnt + 3, 1);
        ST_AT(7);
        tA2(3, lw + 2);
        flag4_wait(lcnt + 32); lds_spin(lcnt + 12, 2);
        tA2(4, lw); tA2(4, lw + 2); tB2(4, lw); tB2(4, lw + 2);
        ST_AT(8);
        WAIT_V(16); ST_AT(9);
      }
      BAR(); ST_AT(10);
      tA2(5, lw); tB2(5, lw); tA2(6, lw); tB2(6, lw);
      if (lw < 2) WAIT_V(16);
      BAR(); tA2(7, lw); tB2(7, lw);
      unsigned mid;
      WAIT_V(16); BAR();
      asm volatile("global_load_dword %0, %1, %2 sc1" : "=v"(mid) : "v"(0u), "s"(c_h1) : "memory");
      if (lw == 2) signal(c_h2, bit8);
      tA2(8, lw); tB2(8, lw);
      WAIT_V(17); BAR();
      tA2(9, lw); tB2(9, lw);
      asm volatile("s_waitcnt vmcnt(16)" : "+v"(mid) :: "memory"); BAR();
      if ((__builtin_amdgcn_readfirstlane(mid) & oth8) != oth8) wait_count(c_h1, oth8);
      tA2(10, lw); tB2(10, lw);
      for (int t = 7; t < 10; ++t) { WAIT_V(16); BAR(); tA2(t + 4, lw); tB2(t + 4, lw); }
      unsigned late;
      WAIT_V(16); BAR();
      asm volatile("global_load_dword %0, %1, %2 sc1" : "=v"(late) : "v"(0u), "s"(c_h2) : "memory");
      tA2(14, lw); tB2(14, lw);
      WAIT_V(16); BAR(); tA2(15, lw); tB2(15, lw);
      asm volatile("s_waitcnt vmcnt(16)" : "+v"(late) :: "memory"); BAR();
      if ((__builtin_amdgcn_readfirstlane(late) & oth8) != oth8) wait_count(c_h2, oth8);
      tA2(16, lw); tB2(16, lw);
      for (int t = 13; t < 24; ++t) { WAIT_V(16); BAR(); tA2(t + 4, lw); tB2(t + 4, lw); }
      WAIT_V(16); BAR();
      WAIT_V(8); BAR();
      WAIT_V(0); BAR();
      BAR();
    }
    if (!LAST) {
      set_m0(lds0, L_JUNK);
      if (lw < 2) touch_line((lw * 64 + lane) * 128, p.wt + (bx * 32 + bi) * 4096);
      int l2 = lane; pin(l2); rowfft_consts(rc, l2 & 15, l2 >> 4); pin_consts(rc);
    }
    __builtin_amdgcn_s_setprio(0);
  } else {
    const int wm = wid & 1, wn = wid >> 1;

    if (wid == 0) wait_count<0>(c_xn, oth16);
    ST_AT(13);
    LBAR();
    ST_AT(3);
    {
      f32x4 acc[7][4];
      M1::run<0>(smem, acc, wm, wn, [&]() { BAR(); }, [&]() {
#pragma unroll
        for (int n = 0; n < 7; ++n) {
          const u32x2 b = *(const LAS u32x2*)(smem + L_BIAS + (wn * 7 + n) * 32 + g * 8);
          const f32x4 bv = {bflo(b.x), bfhi(b.x), bflo(b.y), bfhi(b.y)};
#pragma unroll
          for (int m = 0; m < 4; ++m) acc[n][m] = bv;
        }
      }, [](int) {}, [](int) {});
      ST_AT(4);
      auto gelu4 = [&](f32x4 a) -> u32x2 {
        const float k1 = -2.0f * 1.4426950408889634f * 0.7978845608028654f, k2 = k1 * 0.044715f;
        const f32x2 x0 = {a[0], a[1]}, x1 = {a[2], a[3]};
        f32x2 t0 = x0 * (x0 * x0 * k2 + k1), t1 = x1 * (x1 * x1 * k2 + k1);
        t0.x = __builtin_amdgcn_exp2f(t0.x); t0.y = __builtin_amdgcn_exp2f(t0.y); t1.x = __builtin_amdgcn_exp2f(t1.x); t1.y = __builtin_amdgcn_exp2f(t1.y);
        t0 = t0 + 1.0f; t1 = t1 + 1.0f;
        t0.x = __builtin_amdgcn_rcpf(t0.x); t0.y = __builtin_amdgcn_rcpf(t0.y); t1.x = __builtin_amdgcn_rcpf(t1.x); t1.y = __builtin_amdgcn_rcpf(t1.y);
        return (u32x2){pk2(t0 * x0), pk2(t1 * x1)};
      };
      auto epi = [&](auto WN) {
        constexpr int w = WN;
#pragma unroll
        for (int q = 0; q < 4; ++q) {
#pragma unroll
          for (int m = 0; m < 4; ++m) {
            const int kb = q == 3 ? 3 : w * 4 + q;
            LAS char* lo = smem + L_A + (kb >> 1) * 16384 + (wm * 4 + m) * 2048 + (kb & 1) * 1024 + lane * 16;
            if (q == 3) {
              const u32x2 o = gelu4(acc[w == 0 ? 6 : 0][m]);
              *(LAS u32x2*)(lo + w * 8) = o;
            } else {
              const int n = 2 * q + w;
              const u32x2 a = gelu4(acc[n][m]), b = gelu4(acc[n + 1][m]);
              const u32x4 o = {a.x, a.y, b.x, b.y};
              *(LAS u32x4*)lo = o;
            }
            SCHED();
            if (m == 3) { flag4_set(lcnt + 16 + 4 * q + wid); SCHED(); }
          }
        }
      };
      if (wn == 0) epi(IC<0>{}); else epi(IC<1>{});
      ST_AT(5);
    }
    {
      f32x4 acc[4][4];
      M2::run<2>(smem, acc, wm, wn, [&]() { flag4_wait(lcnt + 28); }, [&]() {
#pragma unroll
        for (int n = 0; n < 4; ++n)
#pragma unroll
          for (int m = 0; m < 4; ++m) acc[n][m] = (f32x4){0.f, 0.f, 0.f, 0.f};
      }, [&](int t) { if (t == 0) flag4_set(lcnt + 32 + wid); }, [&](int it) {
      });
      ST_AT(8);
      const rsrc_t rp = mkrs(p.part + (size_t)(jt >> 3) * S_ * D_ + (size_t)(mt * 16 + wm * 8) * 8192);
#pragma unroll
      for (int m = 0; m < 4; ++m)
#pragma unroll
        for (int q = 0; q < 2; ++q) {
          const f32x4 a = acc[2 * q][m], b = acc[2 * q + 1][m];
          const u32x4 o = {pk(a[0], a[1]), pk(a[2], a[3]), pk(b[0], b[1]), pk(b[2], b[3])};
          st16_dev(rp, (2 * m + (li >> 3)) * 16384 + ((jt & 7) * 4 + wn * 2 + q) * 512 + (li & 7) * 64 + g * 16, o);
        }
      if (drained_last(lcnt + 1, 4)) signal(c_p, bit16);
      ST_AT(9);
      if (!LAST) { int l2 = lane; pin(l2); rowfft_consts(rc, l2 & 15, l2 >> 4); pin_consts(rc); }
    }
  }
  f32x2 xb[2][4];
  unsigned poff[2];
#pragma unroll
  for (int i = 0; i < 2; ++i) {
    const int c = tid + 512 * i, blk = c >> 5, slot = (c & 31) ^ (blk & 7), gg = slot & 3;
    const u32x2 b0 = *(const u32x2*)(p.b2 + 32 * blk + 4 * gg), b1 = *(const u32x2*)(p.b2 + 32 * blk + 16 + 4 * gg);
    const u32x4 x1 = *(const LAS u32x4*)(smem + L_X1 + c * 16), bv = {b0.x, b0.y, b1.x, b1.y};
#pragma unroll
    for (int e = 0; e < 4; ++e) { xb[i][e] = (f32x2){bflo(x1[e]), bfhi(x1[e])} + (f32x2){bflo(bv[e]), bfhi(bv[e])}; pin(xb[i][e]); }
    poff[i] = blk * 512 + slot * 16; pin(poff[i]);
  }
  if (wid == 4) wait_count<0>(c_p, oth16);
  BAR();
  if (wid < 4) ST_AT(10);
  {
    const rsrc_t rp = mkrs(p.part + (size_t)beta * 8192);
    u32x4 a0[2], a1[2];
#pragma unroll
    for (int i = 0; i < 2; ++i) { a0[i] = ld16_dev(rp, poff[i]); a1[i] = __builtin_amdgcn_raw_buffer_load_b128(rp, poff[i], S_ * D_ * 2, 16); }
    LAS char* I = smem + 16640;
#pragma unroll
    for (int i = 0; i < 2; ++i) {
      const int c = tid + 512 * i, blk = c >> 5, slot = (c & 31) ^ (blk & 7), j = slot >> 2, gg = slot & 3;
      u32x4 o;
#pragma unroll
      for (int e = 0; e < 4; ++e) o[e] = pk2(xb[i][e] + ((f32x2){bflo(a0[i][e]), bfhi(a0[i][e])} + (f32x2){bflo(a1[i][e]), bfhi(a1[i][e])}));
      if (LAST) {
        *(LAS u32x2*)(I + j * 2112 + blk * 64 + gg * 8) = (u32x2){o[0], o[1]};
        *(LAS u32x2*)(I + j * 2112 + blk * 64 + 32 + gg * 8) = (u32x2){o[2], o[3]};
      } else {
        *(LAS u32x4*)(I + j * 2112 + blk * 64 + gg * 16) = o;
        st16_out(p.xout + (size_t)(beta * 8 + j) * D_ + blk * 32 + gg * 8, o);
      }
    }
    LBAR();
    ST_AT(11);
    if (LAST) {
      const int s = 256 * (beta >> 5) + 32 * wid + (beta & 31);
#pragma unroll
      for (int i = 0; i < 2; ++i) {
        st16_out(p.xout + (size_t)s * D_ + i * 512 + lane * 8, *(const LAS u32x4*)(I + wid * 2112 + i * 1024 + lane * 16));
      }
    } else {
      const int n2 = 2 * li, pos = 8 * ((n2 >> 2) & 3) + 4 * (n2 >> 4) + (n2 & 3);
      f32x2 v[8];
#pragma unroll
      for (int j = 0; j < 8; ++j) {
        const unsigned u = *(const LAS unsigned*)(I + wid * 2112 + (4 * j + g) * 64 + pos * 2);
        v[j] = (f32x2){bflo(u), bfhi(u)};
      }
      rowfft_run(v, rc, (LAS unsigned*)smem, p.zout + (size_t)beta * 4096, []() {});
    }
  }
  ST_AT(12); ST_FLUSH2();
}

#define ST_FWD
extern "C" __global__ __launch_bounds__(512) void k_block0(BlockArgs p ST_PARAM) { block_body<true, false>(p, CODE_ADDR(k_block0) ST_FWD); }
extern "C" __global__ __launch_bounds__(512) void k_block1(BlockArgs p ST_PARAM) { block_body<false, true>(p, CODE_ADDR(k_block1) ST_FWD); }

extern "C" void kernel_launch(void* const* d_in, const int* in_sizes, int n_in, void* d_out, int out_size, void* d_ws, size_t ws_size,
                              hipStream_t stream) {
  const u16* x = (const u16*)d_in[0];
  char* ws = (char*)d_ws;
  const size_t MB = 1u << 20;
  unsigned* wt = (unsigned*)(ws); unsigned* wq = (unsigned*)(ws + 4 * MB); unsigned* tab = (unsigned*)(ws + 4 * MB + 65536);
  unsigned* cnt = (unsigned*)(ws + 4 * MB + 131072);
  unsigned* z1 = (unsigned*)(ws + 5 * MB); unsigned* z2 = (unsigned*)(ws + 9 * MB);
  u16* x2 = (u16*)(ws + 17 * MB);
  (void)hipFuncSetAttribute((const void*)k_block0, hipFuncAttributeMaxDynamicSharedMemorySize, L_TOTAL);
  (void)hipFuncSetAttribute((const void*)k_block1, hipFuncAttributeMaxDynamicSharedMemorySize, L_TOTAL);
  FirstArgs f; f.x = x; f.wr = (const u16*)d_in[1]; f.wi = (const u16*)d_in[2]; f.z = z1; f.wt = wt; f.wq = wq; f.tab = tab; f.cnt = cnt; f.w1 = (const u16*)d_in[4];
  BlockArgs a; a.zin = z2; a.xin = x; a.tab = tab; a.w1 = (const u16*)d_in[4]; a.b1 = (const u16*)d_in[5]; a.w2 = (const u16*)d_in[6]; a.b2 = (const u16*)d_in[7];
  a.xn = (u16*)(ws + 25 * MB); a.h = (u16*)(ws + 29 * MB); a.part = (u16*)(ws + 44 * MB); a.cnt = cnt; a.xout = x2; a.zout = z1; a.wt = wt;
  k_first<<<FIRST_BLOCKS, 512, 0, stream>>>(f ST_ARG(0));
  k_col<<<256, 512, 0, stream>>>(z1, z2, wt, wq, tab ST_ARG(1));
  k_block0<<<256, 512, L_TOTAL, stream>>>(a ST_ARG(2));
  k_col<<<256, 512, 0, stream>>>(z1, z2, wt, wq, tab ST_ARG(3));
  a.xin = x2; a.cnt = cnt + CNT_WORDS / 2; a.xout = (u16*)d_out;
  k_block1<<<256, 512, L_TOTAL, stream>>>(a ST_ARG(4));
}
```
